```python
import torch

def _softmax(x, axis=-1):
    return torch.softmax(x, dim=axis)
def _gelu(x):
    return torch.nn.functional.gelu(x, approximate="tanh")
def _silu(x):
    return torch.nn.functional.silu(x)
def _sigmoid(x):
    return torch.sigmoid(x)
def _layernorm(x, axis=-1, eps=1e-5):
    m = x.mean(dim=axis, keepdim=True)
    v = x.var(dim=axis, keepdim=True, unbiased=False)
    return (x - m) / torch.sqrt(v + eps)
def _scatter_add(tgt, idx, src, axis=0):
    return tgt.clone().index_add_(axis, idx, src)
def _segment_sum(src, seg, n):
    out = torch.zeros((n,) + src.shape[1:], dtype=src.dtype)
    return out.index_add_(0, seg, src)
def _pad(x, pw):
    flat = tuple(v for pair in reversed(pw) for v in pair)
    return torch.nn.functional.pad(x, flat)

kernel_name = 'concat_heads__gqa_block_cached__mha_block_cached__near_ridge'

def setup_inputs():

    _w = (lambda a: (lambda t: t.to(torch.bfloat16) if t.is_floating_point() else t)(torch.as_tensor(a)))
    def _A_setup():
        B, Sq, D = 16, 48, 512
        Hq, Hkv, Sk, Dh = 32, 8, 1024, 128
        assert Hq % Hkv == 0, (Hq, Hkv)
        return {
            "x": _w((lambda *s: torch.randn(*s, dtype=torch.bfloat16) * 0.8)(B, Sq, D)),
            "k_cache": _w((lambda *s: torch.randn(*s, dtype=torch.bfloat16) * 0.8)(B, Hkv, Sk, Dh)),
            "v_cache": _w((lambda *s: torch.randn(*s, dtype=torch.bfloat16) * 0.05)(B, Hkv, Sk, Dh)),
            "wq": _w((lambda *s: torch.randn(*s, dtype=torch.bfloat16) * 0.8)(D, Hq * Dh) * (D ** -0.5)),
            "wo": _w((lambda *s: torch.randn(*s, dtype=torch.bfloat16) * 0.05)(Hq * Dh, D) * ((Hq * Dh) ** -0.5)),
        }
    _A_in = _A_setup()

    def _B_setup():
        B, Sq, D = 16, 48, 512
        H, Sk, Dh = 32, 1024, 128
        return {
            "x": _w((lambda *s: torch.randn(*s, dtype=torch.bfloat16) * 0.8)(B, Sq, D)),
            "k_cache": _w((lambda *s: torch.randn(*s, dtype=torch.bfloat16) * 0.8)(B, H, Sk, Dh)),
            "v_cache": _w((lambda *s: torch.randn(*s, dtype=torch.bfloat16) * 0.05)(B, H, Sk, Dh)),
            "wq": _w((lambda *s: torch.randn(*s, dtype=torch.bfloat16) * 0.8)(D, H * Dh) * (D ** -0.5)),
            "wo": _w((lambda *s: torch.randn(*s, dtype=torch.bfloat16) * 0.05)(H * Dh, D) * ((H * Dh) ** -0.5)),
        }
    _B_in = _B_setup()

    return {
        "A_x": _A_in["x"],
        "A_k_cache": _A_in["k_cache"],
        "A_v_cache": _A_in["v_cache"],
        "A_wq": _A_in["wq"],
        "A_wo": _A_in["wo"],
        "B_x": _B_in["x"],
        "B_k_cache": _B_in["k_cache"],
        "B_v_cache": _B_in["v_cache"],
        "B_wq": _B_in["wq"],
        "B_wo": _B_in["wo"],
    }

def reference(A_x, A_k_cache, A_v_cache, A_wq, A_wo, B_x, B_k_cache, B_v_cache, B_wq, B_wo):
    def _A_ref(x, k_cache, v_cache, wq, wo):
        B, Sq, D = x.shape
        Hq, Dh = 32, 128
        Hkv = k_cache.shape[1]
        rep = Hq // Hkv
        q = (lambda x, *d: x.permute(*d))((x @ wq).reshape(B, Sq, Hq, Dh), 0, 2, 1, 3)
        k = (lambda x, n, axis: torch.repeat_interleave(x, n, dim=axis))(k_cache, rep, axis=1)
        v = (lambda x, n, axis: torch.repeat_interleave(x, n, dim=axis))(v_cache, rep, axis=1)
        scores = torch.einsum("bhqd,bhkd->bhqk", q, k) * (Dh ** -0.5)
        p = _softmax(scores, axis=-1)
        o = torch.einsum("bhqk,bhkd->bhqd", p, v)
        return (lambda x, *d: x.permute(*d))(o, 0, 2, 1, 3).reshape(B, Sq, Hq * Dh) @ wo

    def _B_ref(x, k_cache, v_cache, wq, wo):
        B, Sq, D = x.shape
        H, Dh = 32, 128
        q = (lambda x, *d: x.permute(*d))((x @ wq).reshape(B, Sq, H, Dh), 0, 2, 1, 3)
        scores = torch.einsum("bhqd,bhkd->bhqk", q, k_cache) * (Dh ** -0.5)
        p = _softmax(scores, axis=-1)
        o = torch.einsum("bhqk,bhkd->bhqd", p, v_cache)
        return (lambda x, *d: x.permute(*d))(o, 0, 2, 1, 3).reshape(B, Sq, H * Dh) @ wo

    a = _A_ref(A_x, A_k_cache, A_v_cache, A_wq, A_wo)
    b = _B_ref(B_x, B_k_cache, B_v_cache, B_wq, B_wo)
    return torch.concatenate([a, b], axis=-1)
```

```cpp
#include <hip/hip_runtime.h>
#include <hip/hip_bf16.h>
#include <stdint.h>

typedef short s16x8 __attribute__((ext_vector_type(8)));
typedef short s16x4 __attribute__((ext_vector_type(4)));
typedef float f32x4 __attribute__((ext_vector_type(4)));
typedef unsigned u32x4 __attribute__((ext_vector_type(4)));
typedef unsigned u32x2 __attribute__((ext_vector_type(2)));
typedef __bf16 bf16x8 __attribute__((ext_vector_type(8)));
typedef __bf16 bf16x2 __attribute__((ext_vector_type(2)));
typedef float f32x2 __attribute__((ext_vector_type(2)));
typedef __attribute__((address_space(3))) s16x4* lds_s16x4_p;
typedef __attribute__((address_space(3))) u32x4* lds_u32x4_p;
typedef __attribute__((address_space(3))) char* lds_char_p;

constexpr int NB = 16, SQ = 48, DM = 512, NH = 32, SK = 1024, DH = 128;
constexpr int STAGE = 49152;
constexpr int NSLOT = 3;
constexpr int NT = SK / 32;
constexpr float SCL2 = 0.08838834764831845f * 1.4426950408889634f;
#define RESCALE_THR 6.0f

__device__ __forceinline__ unsigned pack2(float a, float b) {
  f32x2 v = {a, b};
  bf16x2 r = __builtin_convertvector(v, bf16x2);
  return __builtin_bit_cast(unsigned, r);
}
__device__ __forceinline__ f32x4 mfma16(u32x4 a, u32x4 b, f32x4 c) {
  return __builtin_amdgcn_mfma_f32_16x16x32_bf16(__builtin_bit_cast(bf16x8, a), __builtin_bit_cast(bf16x8, b), c, 0, 0, 0);
}
__device__ __forceinline__ __amdgpu_buffer_rsrc_t mkrsrc(const void* p, unsigned bytes) {
  return __builtin_amdgcn_make_buffer_rsrc((void*)p, 0, bytes, 0x00020000);
}
__device__ __forceinline__ const char* rfl_ptr(const char* p) {
  uint64_t v = (uint64_t)p;
  unsigned lo = __builtin_amdgcn_readfirstlane((unsigned)v), hi = __builtin_amdgcn_readfirstlane((unsigned)(v >> 32));
  return (const char*)(((uint64_t)hi << 32) | lo);
}

__device__ __forceinline__ void dma4(unsigned m0v, __amdgpu_buffer_rsrc_t r, unsigned soff,
                                     unsigned v0, unsigned v1, unsigned v2, unsigned v3) {
  asm volatile(
      "s_mov_b32 m0, %0\n\ts_nop 0\n\t"
      "buffer_load_dwordx4 %1, %5, %6 offen nt lds\n\t"
      "buffer_load_dwordx4 %2, %5, %6 offen offset:1024 nt lds\n\t"
      "buffer_load_dwordx4 %3, %5, %6 offen offset:2048 nt lds\n\t"
      "buffer_load_dwordx4 %4, %5, %6 offen offset:3072 nt lds"
      :: "s"(m0v), "v"(v0), "v"(v1), "v"(v2), "v"(v3), "s"(r), "s"(soff) : "memory");
}

__global__ __launch_bounds__(256, 1) void attn_kernel(
    const char* __restrict__ xA, const char* __restrict__ KA, const char* __restrict__ VA, const char* __restrict__ wqA,
    const char* __restrict__ xB, const char* __restrict__ KB, const char* __restrict__ VB, const char* __restrict__ wqB,
    u32x4* __restrict__ Obuf) {
  extern __shared__ __attribute__((aligned(16))) char smem[];
  const int bid = blockIdx.x, xj = bid & 7, ci = bid >> 3;
  const int w = __builtin_amdgcn_readfirstlane(threadIdx.x >> 6), l = threadIdx.x & 63;
  const int r = l & 15, g = l >> 4;
  const unsigned ldsbase = (unsigned)(uintptr_t)(lds_char_p)smem;

  const int hM = 4 * xj + (ci & 3), bM = 2 * (ci >> 2);
  const int bG = ci >> 1, hG = 4 * xj + 2 * (ci & 1);
  const size_t U = (size_t)SK * DH * 2;

  __amdgpu_buffer_rsrc_t drs[3];
  unsigned dvo[3][4], dso[3], dm0[3];
#pragma unroll
  for (int e = 0; e < 3; e++) {
    const int G = 3 * w + e, st = G >> 2, isV = (G >> 1) & 1, hf = G & 1;
    const char* base = st == 2 ? (isV ? VA : KA) + (size_t)(bG * 8 + xj) * U
                               : (isV ? VB : KB) + (size_t)((bM + st) * NH + hM) * U;
    drs[e] = mkrsrc(rfl_ptr(base), (unsigned)U);
    dso[e] = hf * 4096;
    dm0[e] = ldsbase + G * 4096;
#pragma unroll
    for (int i = 0; i < 4; i++) {
      const int sw = isV ? ((4 * (i & 1) + g) << 1) : (4 * i + g);
      dvo[e][i] = g * 256 + ((r ^ sw) << 4);
    }
  }
  auto issue_group = [&](int e, int t, int slot) {
    dma4(dm0[e] + slot * STAGE, drs[e], dso[e] + t * 8192, dvo[e][0], dvo[e][1], dvo[e][2], dvo[e][3]);
  };

  const int tb = w < 2 ? bM + w : bG, th = w < 2 ? hM : hG + (w - 2);
  u32x4 qf[3][4];
  {
    const char* xp = rfl_ptr((w < 2 ? xB : xA) + (size_t)tb * SQ * DM * 2);
    const char* wp = rfl_ptr((w < 2 ? wqB : wqA) + (size_t)th * DH * 2);
    __amdgpu_buffer_rsrc_t rx = mkrsrc(xp, SQ * DM * 2);
    __amdgpu_buffer_rsrc_t rw = mkrsrc(wp, (DM - 1) * NH * DH * 2 + DH * 2);
    const int xvo = r * (DM * 2) + g * 16;
    const int wvo = g * 8 * (NH * DH * 2) + (4 * (r & 3) + (r >> 2)) * 16;
    constexpr int QD = 4;
    u32x4 wb[QD][8], xb[QD][3];
    f32x4 qa[8][3];
#pragma unroll
    for (int i = 0; i < 8; i++)
#pragma unroll
      for (int qb = 0; qb < 3; qb++) qa[i][qb] = f32x4{0.f, 0.f, 0.f, 0.f};
    auto ldstep = [&](int kk) {
#pragma unroll
      for (int j = 0; j < 8; j++)
        wb[kk % QD][j] = __builtin_amdgcn_raw_buffer_load_b128(rw, wvo, (kk * 32 + j) * (NH * DH * 2), 0);
#pragma unroll
      for (int qb = 0; qb < 3; qb++)
        xb[kk % QD][qb] = __builtin_amdgcn_raw_buffer_load_b128(rx, xvo + kk * 64, qb * 16 * DM * 2, 0);
    };
#pragma unroll
    for (int kk = 0; kk < QD - 1; kk++) { ldstep(kk); __builtin_amdgcn_sched_barrier(0); }
#pragma unroll
    for (int kk = 0; kk < 16; kk++) {
      if (kk + QD - 1 < 16) ldstep(kk + QD - 1);
      if (kk < 9) issue_group(kk % 3, kk / 3, kk / 3);
      __builtin_amdgcn_sched_barrier(0);
#pragma unroll
      for (int c = 0; c < 4; c++) {
        u32x4 lo, hi;
#pragma unroll
        for (int p = 0; p < 4; p++) {
          lo[p] = __builtin_amdgcn_perm(wb[kk % QD][2 * p + 1][c], wb[kk % QD][2 * p][c], 0x05040100u);
          hi[p] = __builtin_amdgcn_perm(wb[kk % QD][2 * p + 1][c], wb[kk % QD][2 * p][c], 0x07060302u);
        }
#pragma unroll
        for (int qb = 0; qb < 3; qb++) {
          qa[2 * c][qb] = mfma16(lo, xb[kk % QD][qb], qa[2 * c][qb]);
          qa[2 * c + 1][qb] = mfma16(hi, xb[kk % QD][qb], qa[2 * c + 1][qb]);
        }
      }
      __builtin_amdgcn_sched_barrier(0);
    }
#pragma unroll
    for (int qb = 0; qb < 3; qb++)
#pragma unroll
      for (int s = 0; s < 4; s++)
#pragma unroll
        for (int p = 0; p < 4; p++) qf[qb][s][p] = pack2(qa[2 * p][qb][s], qa[2 * p + 1][qb][s]);
  }

  const int strm = w < 2 ? w : 2;
  unsigned kaddr[4], vaddr0;
#pragma unroll
  for (int s = 0; s < 4; s++) kaddr[s] = strm * 16384 + r * 256 + (((4 * s + g) ^ r) << 4);
  {
    const int row = 4 * g + (r >> 2), p = r & 3;
    vaddr0 = strm * 16384 + 8192 + row * 256 + ((((p >> 1)) ^ ((row & 7) << 1)) << 4) + (p & 1) * 8;
  }
  f32x4 oa[8][3];
#pragma unroll
  for (int i = 0; i < 8; i++)
#pragma unroll
    for (int qb = 0; qb < 3; qb++) oa[i][qb] = f32x4{0.f, 0.f, 0.f, 0.f};
  float mrow[3] = {-INFINITY, -INFINITY, -INFINITY}, lsum[3] = {0.f, 0.f, 0.f};

  int slot = 0;
  for (int t = 0; t < NT; t++) {
    if (t == 0) asm volatile("s_waitcnt vmcnt(24) lgkmcnt(0)\n\ts_barrier" ::: "memory");
    else if (t == NT - 1) asm volatile("s_waitcnt vmcnt(0) lgkmcnt(0)\n\ts_barrier" ::: "memory");
    else asm volatile("s_waitcnt vmcnt(12) lgkmcnt(0)\n\ts_barrier" ::: "memory");
    if (t >= 1 && t + 2 < NT) {
      const int fs = slot == 0 ? 2 : slot - 1;
#pragma unroll
      for (int e = 0; e < 3; e++) issue_group(e, t + 2, fs);
    }
    const char* sb = smem + slot * STAGE;
    f32x4 sa[2][3];
#pragma unroll
    for (int kb = 0; kb < 2; kb++) {
      u32x4 kf[4];
#pragma unroll
      for (int s = 0; s < 4; s++) kf[s] = *(const u32x4*)(sb + kaddr[s] + kb * 4096);
#pragma unroll
      for (int qb = 0; qb < 3; qb++) {
        f32x4 a = {0.f, 0.f, 0.f, 0.f};
#pragma unroll
        for (int s = 0; s < 4; s++) a = mfma16(kf[s], qf[qb][s], a);
        sa[kb][qb] = a;
      }
    }
    float mx[3];
    bool need = false;
#pragma unroll
    for (int qb = 0; qb < 3; qb++) {
      float m0 = fmaxf(fmaxf(sa[0][qb][0], sa[0][qb][1]), fmaxf(sa[0][qb][2], sa[0][qb][3]));
      float m1 = fmaxf(fmaxf(sa[1][qb][0], sa[1][qb][1]), fmaxf(sa[1][qb][2], sa[1][qb][3]));
      mx[qb] = fmaxf(m0, m1);
      need |= mx[qb] > mrow[qb] + RESCALE_THR;
    }
    if (__any(need)) {
#pragma unroll
      for (int qb = 0; qb < 3; qb++) {
        float m = mx[qb];
        m = fmaxf(m, __shfl_xor(m, 16));
        m = fmaxf(m, __shfl_xor(m, 32));
        m = fmaxf(m, mrow[qb]);
        const float alpha = __builtin_amdgcn_exp2f((mrow[qb] - m) * SCL2);
        mrow[qb] = m;
        lsum[qb] *= alpha;
#pragma unroll
        for (int i = 0; i < 8; i++) oa[i][qb] *= alpha;
      }
    }
    u32x4 pf[3];
#pragma unroll
    for (int qb = 0; qb < 3; qb++) {
      const float mc = mrow[qb] * SCL2;
      float p[8];
#pragma unroll
      for (int kb = 0; kb < 2; kb++)
#pragma unroll
        for (int e = 0; e < 4; e++) p[kb * 4 + e] = __builtin_amdgcn_exp2f(__builtin_fmaf(sa[kb][qb][e], SCL2, -mc));
      lsum[qb] += ((p[0] + p[1]) + (p[2] + p[3])) + ((p[4] + p[5]) + (p[6] + p[7]));
#pragma unroll
      for (int e = 0; e < 4; e++) pf[qb][e] = pack2(p[2 * e], p[2 * e + 1]);
    }
#pragma unroll
    for (int db = 0; db < 8; db++) {
      const char* va = sb + (vaddr0 ^ (db << 5));
      s16x4 v0 = __builtin_amdgcn_ds_read_tr16_b64_v4i16((lds_s16x4_p)(va));
      s16x4 v1 = __builtin_amdgcn_ds_read_tr16_b64_v4i16((lds_s16x4_p)(va + 4096));
      u32x2 a0 = __builtin_bit_cast(u32x2, v0), a1 = __builtin_bit_cast(u32x2, v1);
      u32x4 vf = {a0[0], a0[1], a1[0], a1[1]};
#pragma unroll
      for (int qb = 0; qb < 3; qb++) oa[db][qb] = mfma16(vf, pf[qb], oa[db][qb]);
    }
    slot = slot == 2 ? 0 : slot + 1;
  }

  const int task = ((w < 2 ? 1 : 0) * NB + tb) * NH + th;
#pragma unroll
  for (int qb = 0; qb < 3; qb++) {
    float s = lsum[qb];
    s += __shfl_xor(s, 16);
    s += __shfl_xor(s, 32);
    const float inv = 1.0f / s;
#pragma unroll
    for (int u = 0; u < 4; u++) {
      u32x4 o;
      o[0] = pack2(oa[2 * u][qb][0] * inv, oa[2 * u][qb][1] * inv);
      o[1] = pack2(oa[2 * u][qb][2] * inv, oa[2 * u][qb][3] * inv);
      o[2] = pack2(oa[2 * u + 1][qb][0] * inv, oa[2 * u + 1][qb][1] * inv);
      o[3] = pack2(oa[2 * u + 1][qb][2] * inv, oa[2 * u + 1][qb][3] * inv);
      Obuf[((task * 4 + u) * 3 + qb) * 64 + l] = o;
    }
  }
}

__global__ __launch_bounds__(512, 2) void oproj_kernel(const u32x4* __restrict__ Obuf, const char* __restrict__ woA,
                                                       const char* __restrict__ woB, char* __restrict__ out) {
  extern __shared__ __attribute__((aligned(16))) char smem[];
  const int bid = blockIdx.x, ns = bid & 7, b = (bid >> 3) & 15, br = bid >> 7;
  const int w = __builtin_amdgcn_readfirstlane(threadIdx.x >> 6), l = threadIdx.x & 63;
  const int r = l & 15, g = l >> 4;
  const char* wp = rfl_ptr((br ? woB : woA) + (size_t)(w * 512) * (DM * 2) + ns * 128);
  __amdgpu_buffer_rsrc_t rw = mkrsrc(wp, 511 * DM * 2 + 128);
  const char* op = rfl_ptr((const char*)(Obuf + (size_t)((br * NB + b) * NH + w * 4) * 12 * 64));
  __amdgpu_buffer_rsrc_t ro = mkrsrc(op, 4 * 12 * 1024);
  const int wvo = 4 * g * (DM * 2) + r * 8;
  constexpr int QD = 4;
  u32x2 wb[QD][8];
  u32x4 ob[QD][3];
  f32x4 acc[4][3];
#pragma unroll
  for (int i = 0; i < 4; i++)
#pragma unroll
    for (int qb = 0; qb < 3; qb++) acc[i][qb] = f32x4{0.f, 0.f, 0.f, 0.f};
  auto ldstep = [&](int kk) {
#pragma unroll
    for (int j = 0; j < 8; j++)
      wb[kk % QD][j] = __builtin_amdgcn_raw_buffer_load_b64(rw, wvo, (kk * 32 + (j & 3) + 16 * (j >> 2)) * (DM * 2), 0);
#pragma unroll
    for (int qb = 0; qb < 3; qb++)
      ob[kk % QD][qb] = __builtin_amdgcn_raw_buffer_load_b128(ro, l * 16, (kk * 3 + qb) * 1024, 0);
  };
#pragma unroll
  for (int kk = 0; kk < QD - 1; kk++) { ldstep(kk); __builtin_amdgcn_sched_barrier(0); }
#pragma unroll
  for (int kk = 0; kk < 16; kk++) {
    if (kk + QD - 1 < 16) ldstep(kk + QD - 1);
    __builtin_amdgcn_sched_barrier(0);
#pragma unroll
    for (int c = 0; c < 2; c++) {
      u32x4 lo, hi;
#pragma unroll
      for (int p = 0; p < 4; p++) {
        lo[p] = __builtin_amdgcn_perm(wb[kk % QD][2 * p + 1][c], wb[kk % QD][2 * p][c], 0x05040100u);
        hi[p] = __builtin_amdgcn_perm(wb[kk % QD][2 * p + 1][c], wb[kk % QD][2 * p][c], 0x07060302u);
      }
#pragma unroll
      for (int qb = 0; qb < 3; qb++) {
        acc[2 * c][qb] = mfma16(lo, ob[kk % QD][qb], acc[2 * c][qb]);
        acc[2 * c + 1][qb] = mfma16(hi, ob[kk % QD][qb], acc[2 * c + 1][qb]);
      }
    }
    __builtin_amdgcn_sched_barrier(0);
  }
  f32x4* red = (f32x4*)smem;
#pragma unroll
  for (int qb = 0; qb < 3; qb++)
#pragma unroll
    for (int rho = 0; rho < 4; rho++)
      red[((w * 3 + qb) * 4 + rho) * 64 + l] = f32x4{acc[0][qb][rho], acc[1][qb][rho], acc[2][qb][rho], acc[3][qb][rho]};
  __syncthreads();
  if (w < 6) {
    const int qb = w >> 1, rp = w & 1;
    f32x4 s0 = {0.f, 0.f, 0.f, 0.f}, s1 = s0;
#pragma unroll
    for (int ww = 0; ww < 8; ww++) {
      s0 += red[((ww * 3 + qb) * 4 + 2 * rp) * 64 + l];
      s1 += red[((ww * 3 + qb) * 4 + 2 * rp + 1) * 64 + l];
    }
    u32x4 o = {pack2(s0[0], s0[1]), pack2(s0[2], s0[3]), pack2(s1[0], s1[1]), pack2(s1[2], s1[3])};
    *(u32x4*)(out + (size_t)(b * SQ + qb * 16 + r) * 2048 + br * 1024 + ns * 128 + g * 32 + rp * 16) = o;
  }
}

extern "C" void kernel_launch(void* const* d_in, const int* in_sizes, int n_in, void* d_out, int out_size,
                              void* d_ws, size_t ws_size, hipStream_t stream) {
  const char* xA = (const char*)d_in[0]; const char* KA = (const char*)d_in[1]; const char* VA = (const char*)d_in[2];
  const char* wqA = (const char*)d_in[3]; const char* woA = (const char*)d_in[4];
  const char* xB = (const char*)d_in[5]; const char* KB = (const char*)d_in[6]; const char* VB = (const char*)d_in[7];
  const char* wqB = (const char*)d_in[8]; const char* woB = (const char*)d_in[9];
  u32x4* Obuf = (u32x4*)d_ws;
  (void)hipFuncSetAttribute((const void*)attn_kernel, hipFuncAttributeMaxDynamicSharedMemorySize, NSLOT * STAGE);
  (void)hipFuncSetAttribute((const void*)oproj_kernel, hipFuncAttributeMaxDynamicSharedMemorySize, 98304);
  attn_kernel<<<256, 256, NSLOT * STAGE, stream>>>(xA, KA, VA, wqA, xB, KB, VB, wqB, Obuf);
  oproj_kernel<<<256, 512, 98304, stream>>>(Obuf, woA, woB, (char*)d_out);
}
```

```cpp
#include <hip/hip_runtime.h>
#include <hip/hip_bf16.h>
#include <stdint.h>

typedef short s16x4 __attribute__((ext_vector_type(4)));
typedef float f32x4 __attribute__((ext_vector_type(4)));
typedef unsigned u32x4 __attribute__((ext_vector_type(4)));
typedef unsigned u32x2 __attribute__((ext_vector_type(2)));
typedef __bf16 bf16x8 __attribute__((ext_vector_type(8)));
typedef __bf16 bf16x2 __attribute__((ext_vector_type(2)));
typedef float f32x2 __attribute__((ext_vector_type(2)));
typedef __attribute__((address_space(3))) s16x4* lds_s16x4_p;
typedef __attribute__((address_space(3))) char* lds_char_p;

constexpr int NB = 16, SQ = 48, DM = 512, NH = 32, SK = 1024, DH = 128;
constexpr int STAGE = 49152;
constexpr int NSLOT = 3;
constexpr int NT = SK / 32;
constexpr int ATTN_LDS = NSLOT * STAGE + 16384;
constexpr float SCL2 = 0.08838834764831845f * 1.4426950408889634f;
constexpr float RESCALE_THR = 8.0f / SCL2;
#define DMA_ORDER 0

#define STAMP(k, i)

__device__ __forceinline__ unsigned pack2(float a, float b) {
  f32x2 v = {a, b};
  bf16x2 r = __builtin_convertvector(v, bf16x2);
  return __builtin_bit_cast(unsigned, r);
}
__device__ __forceinline__ f32x4 mfma16(u32x4 a, u32x4 b, f32x4 c) {
  return __builtin_amdgcn_mfma_f32_16x16x32_bf16(__builtin_bit_cast(bf16x8, a), __builtin_bit_cast(bf16x8, b), c, 0, 0, 0);
}
__device__ __forceinline__ __amdgpu_buffer_rsrc_t mkrsrc(const void* p, unsigned bytes) {
  return __builtin_amdgcn_make_buffer_rsrc((void*)p, 0, bytes, 0x00020000);
}
__device__ __forceinline__ const char* rfl_ptr(const char* p) {
  uint64_t v = (uint64_t)p;
  unsigned lo = __builtin_amdgcn_readfirstlane((unsigned)v), hi = __builtin_amdgcn_readfirstlane((unsigned)(v >> 32));
  return (const char*)(((uint64_t)hi << 32) | lo);
}
__device__ __forceinline__ u32x4 tr_frag(const char* p, int hi_off) {
  s16x4 v0 = __builtin_amdgcn_ds_read_tr16_b64_v4i16((lds_s16x4_p)(p));
  s16x4 v1 = __builtin_amdgcn_ds_read_tr16_b64_v4i16((lds_s16x4_p)(p + hi_off));
  u32x2 a0 = __builtin_bit_cast(u32x2, v0), a1 = __builtin_bit_cast(u32x2, v1);
  return u32x4{a0[0], a0[1], a1[0], a1[1]};
}

__device__ __forceinline__ void dma4(unsigned m0v, __amdgpu_buffer_rsrc_t r, unsigned soff,
                                     unsigned v0, unsigned v1, unsigned v2, unsigned v3) {
  asm volatile(
      "s_mov_b32 m0, %0\n\ts_nop 0\n\t"
      "buffer_load_dwordx4 %1, %5, %6 offen nt lds\n\t"
      "buffer_load_dwordx4 %2, %5, %6 offen offset:1024 nt lds\n\t"
      "buffer_load_dwordx4 %3, %5, %6 offen offset:2048 nt lds\n\t"
      "buffer_load_dwordx4 %4, %5, %6 offen offset:3072 nt lds"
      :: "s"(m0v), "v"(v0), "v"(v1), "v"(v2), "v"(v3), "s"(r), "s"(soff) : "memory");
}
__device__ __forceinline__ void scale_acc(f32x4& x, float alpha) {
  asm volatile("" : "+a"(x));
  x *= alpha;
  asm volatile("" : "+a"(x));
}

__global__ __launch_bounds__(256, 1) void attn_kernel(
    const char* __restrict__ xA, const char* __restrict__ KA, const char* __restrict__ VA, const char* __restrict__ wqA,
    const char* __restrict__ xB, const char* __restrict__ KB, const char* __restrict__ VB, const char* __restrict__ wqB,
    u32x4* __restrict__ Obuf) {
  extern __shared__ __attribute__((aligned(16))) char smem[];
  const int bid = blockIdx.x, xj = bid & 7, ci = bid >> 3;
  const int w = __builtin_amdgcn_readfirstlane(threadIdx.x >> 6), l = threadIdx.x & 63;
  const int r = l & 15, g = l >> 4;
  const unsigned ldsbase = (unsigned)(uintptr_t)(lds_char_p)smem;
  const int wid = bid * 4 + w; (void)wid;
  STAMP(0, 0);

  const int hM = 4 * xj + (ci & 3), bM = 2 * (ci >> 2);
  const int bG = ci >> 1, hG = 4 * xj + 2 * (ci & 1);
  const size_t U = (size_t)SK * DH * 2;

  __amdgpu_buffer_rsrc_t drs[3];
  unsigned dvo[3][4], dso[3], dm0[3];
#pragma unroll
  for (int e = 0; e < 3; e++) {
    const int G = 3 * w + e, st = G >> 2, isV = (G >> 1) & 1, hf = G & 1;
    const char* base = st == 2 ? (isV ? VA : KA) + (size_t)(bG * 8 + xj) * U
                               : (isV ? VB : KB) + (size_t)((bM + st) * NH + hM) * U;
    drs[e] = mkrsrc(rfl_ptr(base), (unsigned)U);
    dso[e] = hf * 4096;
    dm0[e] = ldsbase + G * 4096;
#pragma unroll
    for (int i = 0; i < 4; i++) {
      const int sw = isV ? ((4 * (i & 1) + g) << 1) : (4 * g + i);
      dvo[e][i] = g * 256 + ((r ^ sw) << 4);
    }
  }
  auto issue_group = [&](int e, int t, int slot) {
    dma4(dm0[e] + slot * STAGE, drs[e], dso[e] + t * 8192, dvo[e][0], dvo[e][1], dvo[e][2], dvo[e][3]);
  };
  if (DMA_ORDER == 1) {
#pragma unroll
    for (int kk = 0; kk < 9; kk++) issue_group(kk % 3, kk / 3, kk / 3);
  }

  const int tb = w < 2 ? bM + w : bG, th = w < 2 ? hM : hG + (w - 2);
  u32x4 qf[3][4];
  {
    const char* xp = rfl_ptr((w < 2 ? xB : xA) + (size_t)tb * SQ * DM * 2);
    const char* wp = rfl_ptr((w < 2 ? wqB : wqA) + (size_t)th * DH * 2);
    __amdgpu_buffer_rsrc_t rx = mkrsrc(xp, SQ * DM * 2);
    __amdgpu_buffer_rsrc_t rw = mkrsrc(wp, (DM - 1) * NH * DH * 2 + DH * 2);
    const int xvo = g * (DM * 2) + r * 16;
    const int wvo = g * 8 * (NH * DH * 2) + r * 16;
    char* xs = smem + NSLOT * STAGE + w * 4096;
    constexpr int QD = 4;
    u32x4 wb[QD][8], xr[3][4], xf[2][3][4];
    f32x4 qa[8][3];
#pragma unroll
    for (int i = 0; i < 8; i++)
#pragma unroll
      for (int qb = 0; qb < 3; qb++) qa[i][qb] = f32x4{0.f, 0.f, 0.f, 0.f};
    auto ldw = [&](int kk) {
#pragma unroll
      for (int j = 0; j < 8; j++)
        wb[kk % QD][j] = __builtin_amdgcn_raw_buffer_load_b128(rw, wvo, (kk * 32 + j) * (NH * DH * 2), 0);
    };
    auto ldx = [&](int B) {
#pragma unroll
      for (int qb = 0; qb < 3; qb++)
#pragma unroll
        for (int i = 0; i < 4; i++)
          xr[qb][i] = __builtin_amdgcn_raw_buffer_load_b128(rx, xvo + B * 256, (qb * 16 + 4 * i) * (DM * 2), 0);
    };
    auto cvtx = [&](int B) {
#pragma unroll
      for (int qb = 0; qb < 3; qb++) {
#pragma unroll
        for (int i = 0; i < 4; i++) *(u32x4*)(xs + (4 * i + g) * 256 + ((r ^ (4 * i + g)) << 4)) = xr[qb][i];
#pragma unroll
        for (int i = 0; i < 4; i++) xf[B & 1][qb][i] = *(const u32x4*)(xs + r * 256 + (((4 * i + g) ^ r) << 4));
      }
    };
    ldx(0);
#pragma unroll
    for (int kk = 0; kk < QD - 1; kk++) ldw(kk);
    __builtin_amdgcn_sched_barrier(0);
    if (DMA_ORDER == 3) {
#pragma unroll
      for (int kk = 0; kk < 9; kk++) issue_group(kk % 3, kk / 3, kk / 3);
    }
    cvtx(0);
    __builtin_amdgcn_sched_barrier(0);
#pragma unroll
    for (int kk = 0; kk < 16; kk++) {
      if (kk + QD - 1 < 16) ldw(kk + QD - 1);
      if ((kk & 3) == 0 && kk < 12) ldx(kk / 4 + 1);
      if (DMA_ORDER == 0 && kk < 9) issue_group(kk % 3, kk / 3, kk / 3);
      if (DMA_ORDER == 4 && (kk == 0 || kk == 6 || kk == 12)) {
#pragma unroll
        for (int e = 0; e < 3; e++) issue_group(e, kk / 6, kk / 6);
      }
      if ((kk & 3) == 3 && kk < 12) cvtx(kk / 4 + 1);
      __builtin_amdgcn_sched_barrier(0);
#pragma unroll
      for (int c = 0; c < 4; c++) {
        u32x4 lo, hi;
#pragma unroll
        for (int p = 0; p < 4; p++) {
          lo[p] = __builtin_amdgcn_perm(wb[kk % QD][2 * p + 1][c], wb[kk % QD][2 * p][c], 0x05040100u);
          hi[p] = __builtin_amdgcn_perm(wb[kk % QD][2 * p + 1][c], wb[kk % QD][2 * p][c], 0x07060302u);
        }
#pragma unroll
        for (int qb = 0; qb < 3; qb++) {
          qa[2 * c][qb] = mfma16(lo, xf[(kk >> 2) & 1][qb][kk & 3], qa[2 * c][qb]);
          qa[2 * c + 1][qb] = mfma16(hi, xf[(kk >> 2) & 1][qb][kk & 3], qa[2 * c + 1][qb]);
        }
      }
      __builtin_amdgcn_sched_barrier(0);
    }
#pragma unroll
    for (int qb = 0; qb < 3; qb++)
#pragma unroll
      for (int s = 0; s < 4; s++)
#pragma unroll
        for (int p = 0; p < 4; p++) qf[qb][s][p] = pack2(qa[2 * p][qb][s], qa[2 * p + 1][qb][s]);
  }
  STAMP(0, 1);
  if (DMA_ORDER == 2) {
#pragma unroll
    for (int kk = 0; kk < 9; kk++) issue_group(kk % 3, kk / 3, kk / 3);
  }

  const int strm = w < 2 ? w : 2;
  unsigned kaddr[4], vaddr0;
#pragma unroll
  for (int s = 0; s < 4; s++) kaddr[s] = strm * 16384 + r * 256 + (((4 * g + s) ^ (4 * (r & 3) + (r >> 2))) << 4);
  {
    const int row = 4 * g + (r >> 2), p = r & 3;
    vaddr0 = strm * 16384 + 8192 + row * 256 + ((((p >> 1)) ^ ((row & 7) << 1)) << 4) + (p & 1) * 8;
  }
  f32x4 oa[8][3];
#pragma unroll
  for (int i = 0; i < 8; i++)
#pragma unroll
    for (int qb = 0; qb < 3; qb++) oa[i][qb] = f32x4{0.f, 0.f, 0.f, 0.f};
  float mrow[3] = {-INFINITY, -INFINITY, -INFINITY}, lsum[3] = {0.f, 0.f, 0.f};

  asm volatile("s_waitcnt vmcnt(24) lgkmcnt(0)\n\ts_barrier" ::: "memory");
  int slot = 0;
  for (int t = 0; t < NT; t++) {
    const bool refill = t >= 1 && t + 2 < NT;
    const int fs = slot == 0 ? 2 : slot - 1;
    const char* sb = smem + slot * STAGE;
    u32x4 kf[2][4], vf[8];
#pragma unroll
    for (int s = 0; s < 4; s++)
#pragma unroll
      for (int kb = 0; kb < 2; kb++) kf[kb][s] = *(const u32x4*)(sb + kaddr[s] + kb * 4096);
    if (refill) issue_group(0, t + 2, fs);
    __builtin_amdgcn_sched_barrier(0);
    f32x4 sa[2][3];
#pragma unroll
    for (int s = 0; s < 4; s++) {
#pragma unroll
      for (int kb = 0; kb < 2; kb++)
#pragma unroll
        for (int qb = 0; qb < 3; qb++)
          sa[kb][qb] = mfma16(kf[kb][s], qf[qb][s], s == 0 ? f32x4{0.f, 0.f, 0.f, 0.f} : sa[kb][qb]);
      vf[2 * s] = tr_frag(sb + (vaddr0 ^ ((2 * s) << 5)), 4096);
      vf[2 * s + 1] = tr_frag(sb + (vaddr0 ^ ((2 * s + 1) << 5)), 4096);
      __builtin_amdgcn_sched_barrier(0);
    }
    if (refill) issue_group(1, t + 2, fs);
    float mx[3];
    bool need = false;
#pragma unroll
    for (int qb = 0; qb < 3; qb++) {
      float m0 = __builtin_fmaxf(__builtin_fmaxf(sa[0][qb][0], sa[0][qb][1]), sa[0][qb][2]);
      m0 = __builtin_fmaxf(__builtin_fmaxf(m0, sa[0][qb][3]), sa[1][qb][0]);
      m0 = __builtin_fmaxf(__builtin_fmaxf(m0, sa[1][qb][1]), sa[1][qb][2]);
      mx[qb] = __builtin_fmaxf(m0, sa[1][qb][3]);
      need |= mx[qb] > mrow[qb] + RESCALE_THR;
    }
    if (__builtin_expect(__builtin_amdgcn_ballot_w64(need) != 0, 0)) {
#pragma unroll
      for (int qb = 0; qb < 3; qb++) {
        float m = mx[qb];
        m = fmaxf(m, __shfl_xor(m, 16));
        m = fmaxf(m, __shfl_xor(m, 32));
        m = fmaxf(m, mrow[qb]);
        const float alpha = __builtin_amdgcn_exp2f((mrow[qb] - m) * SCL2);
        mrow[qb] = m;
        lsum[qb] *= alpha;
#pragma unroll
        for (int i = 0; i < 8; i++) scale_acc(oa[i][qb], alpha);
      }
    }
    u32x4 pf[3];
#pragma unroll
    for (int qb = 0; qb < 3; qb++) {
      const float mc = mrow[qb] * SCL2;
      float p[8];
#pragma unroll
      for (int kb = 0; kb < 2; kb++)
#pragma unroll
        for (int e = 0; e < 4; e++) p[kb * 4 + e] = __builtin_amdgcn_exp2f(__builtin_fmaf(sa[kb][qb][e], SCL2, -mc));
      lsum[qb] += ((p[0] + p[1]) + (p[2] + p[3])) + ((p[4] + p[5]) + (p[6] + p[7]));
#pragma unroll
      for (int e = 0; e < 4; e++) pf[qb][e] = pack2(p[2 * e], p[2 * e + 1]);
    }
    if (refill) issue_group(2, t + 2, fs);
#pragma unroll
    for (int qb = 0; qb < 3; qb++)
#pragma unroll
      for (int db = 0; db < 8; db++) oa[db][qb] = mfma16(vf[db], pf[qb], oa[db][qb]);
    slot = slot == 2 ? 0 : slot + 1;
    if (t >= NT - 2) asm volatile("s_waitcnt vmcnt(0) lgkmcnt(0)\n\ts_barrier" ::: "memory");
    else asm volatile("s_waitcnt vmcnt(12) lgkmcnt(0)\n\ts_barrier" ::: "memory");
  }
  STAMP(0, 10);

  const int task = ((w < 2 ? 1 : 0) * NB + tb) * NH + th;
#pragma unroll
  for (int qb = 0; qb < 3; qb++) {
    float s = lsum[qb];
    s += __shfl_xor(s, 16);
    s += __shfl_xor(s, 32);
    const float inv = 1.0f / s;
#pragma unroll
    for (int u = 0; u < 4; u++) {
      u32x4 o;
      o[0] = pack2(oa[2 * u][qb][0] * inv, oa[2 * u][qb][1] * inv);
      o[1] = pack2(oa[2 * u][qb][2] * inv, oa[2 * u][qb][3] * inv);
      o[2] = pack2(oa[2 * u + 1][qb][0] * inv, oa[2 * u + 1][qb][1] * inv);
      o[3] = pack2(oa[2 * u + 1][qb][2] * inv, oa[2 * u + 1][qb][3] * inv);
      Obuf[((task * 4 + u) * 3 + qb) * 64 + l] = o;
    }
  }
  STAMP(0, 11);
}

__global__ __launch_bounds__(512, 2) void oproj_kernel(const u32x4* __restrict__ Obuf, const char* __restrict__ woA,
                                                       const char* __restrict__ woB, char* __restrict__ out) {
  extern __shared__ __attribute__((aligned(16))) char smem[];
  const int bid = blockIdx.x, xj = bid & 7, ci = bid >> 3;
  const int br = xj >> 2, b = 8 * ((xj >> 1) & 1) + (ci >> 2), ns = 4 * (xj & 1) + (ci & 3);
  const int w = __builtin_amdgcn_readfirstlane(threadIdx.x >> 6), l = threadIdx.x & 63;
  const int r = l & 15, g = l >> 4;
  const int wid = bid * 4 + (w & 3); (void)wid;
  if (w < 4) STAMP(1, 0);
  const unsigned ring = (unsigned)(uintptr_t)(lds_char_p)smem + w * 16384;
  const char* wp = rfl_ptr((br ? woB : woA) + (size_t)(w * 512) * (DM * 2) + ns * 128);
  __amdgpu_buffer_rsrc_t rw = mkrsrc(wp, 511 * DM * 2 + 128);
  const char* op = rfl_ptr((const char*)(Obuf + (size_t)((br * NB + b) * NH + w * 4) * 12 * 64));
  __amdgpu_buffer_rsrc_t ro = mkrsrc(op, 4 * 12 * 1024);
  unsigned wvo[4];
#pragma unroll
  for (int i = 0; i < 4; i++) wvo[i] = (8 * i + (l >> 3)) * (DM * 2) - 1024 * i + (((l & 7) ^ (((l >> 4) & 3) << 1)) << 4);
  const unsigned ovo = l * 16;
  constexpr int QD = 4;
  u32x4 ob[QD][3];
  f32x4 acc[4][3];
#pragma unroll
  for (int i = 0; i < 4; i++)
#pragma unroll
    for (int qb = 0; qb < 3; qb++) acc[i][qb] = f32x4{0.f, 0.f, 0.f, 0.f};
  auto ldstep = [&](int kk) {
    asm volatile(
        "s_mov_b32 m0, %3\n\ts_nop 0\n\t"
        "buffer_load_dwordx4 %4, %8, %9 offen lds\n\t"
        "buffer_load_dwordx4 %5, %8, %9 offen offset:1024 lds\n\t"
        "buffer_load_dwordx4 %6, %8, %9 offen offset:2048 lds\n\t"
        "buffer_load_dwordx4 %7, %8, %9 offen offset:3072 lds\n\t"
        "buffer_load_dwordx4 %0, %10, %11, %12 offen\n\t"
        "buffer_load_dwordx4 %1, %10, %11, %12 offen offset:1024\n\t"
        "buffer_load_dwordx4 %2, %10, %11, %12 offen offset:2048"
        : "=&v"(ob[kk % QD][0]), "=&v"(ob[kk % QD][1]), "=&v"(ob[kk % QD][2])
        : "s"(ring + (kk % QD) * 4096), "v"(wvo[0]), "v"(wvo[1]), "v"(wvo[2]), "v"(wvo[3]), "s"(rw), "s"(kk * 32 * DM * 2),
          "v"(ovo), "s"(ro), "s"(kk * 3072)
        : "memory");
  };
  const int row = 4 * g + (r >> 2), p = r & 3;
  const unsigned aoff = w * 16384 + row * 128 + (((p >> 1) ^ (((row >> 1) & 3) << 1)) << 4) + (p & 1) * 8;
#pragma unroll
  for (int kk = 0; kk < QD - 1; kk++) ldstep(kk);
#pragma unroll
  for (int kk = 0; kk < 16; kk++) {
    if (kk + QD - 1 < 16) {
      asm volatile("s_waitcnt lgkmcnt(0)" ::: "memory");
      ldstep(kk + QD - 1);
      asm volatile("s_waitcnt vmcnt(21)" : "+v"(ob[kk % QD][0]), "+v"(ob[kk % QD][1]), "+v"(ob[kk % QD][2]) :: "memory");
    } else if (kk == 13) {
      asm volatile("s_waitcnt vmcnt(14)" : "+v"(ob[kk % QD][0]), "+v"(ob[kk % QD][1]), "+v"(ob[kk % QD][2]) :: "memory");
    } else if (kk == 14) {
      asm volatile("s_waitcnt vmcnt(7)" : "+v"(ob[kk % QD][0]), "+v"(ob[kk % QD][1]), "+v"(ob[kk % QD][2]) :: "memory");
    } else {
      asm volatile("s_waitcnt vmcnt(0)" : "+v"(ob[kk % QD][0]), "+v"(ob[kk % QD][1]), "+v"(ob[kk % QD][2]) :: "memory");
    }
    u32x4 af[4];
#pragma unroll
    for (int db = 0; db < 4; db++) af[db] = tr_frag(smem + (kk % QD) * 4096 + (aoff ^ (db << 5)), 2048);
#pragma unroll
    for (int db = 0; db < 4; db++)
#pragma unroll
      for (int qb = 0; qb < 3; qb++) acc[db][qb] = mfma16(af[db], ob[kk % QD][qb], acc[db][qb]);
    __builtin_amdgcn_sched_barrier(0);
  }
  if (w < 4) STAMP(1, 1);
  __syncthreads();
  f32x4* red = (f32x4*)smem;
#pragma unroll
  for (int qb = 0; qb < 3; qb++)
#pragma unroll
    for (int db = 0; db < 4; db++) red[((w * 3 + qb) * 4 + db) * 64 + l] = acc[db][qb];
  __syncthreads();
  if (w < 6) {
    const int q = 8 * w + (l >> 3), c8 = l & 7;
    const int qb = q >> 4, idx = (qb * 4 + (c8 >> 1)) * 64 + (2 * (c8 & 1)) * 16 + (q & 15);
    f32x4 s0 = {0.f, 0.f, 0.f, 0.f}, s1 = s0;
#pragma unroll
    for (int ww = 0; ww < 8; ww++) {
      s0 += red[ww * 768 + idx];
      s1 += red[ww * 768 + idx + 16];
    }
    u32x4 o = {pack2(s0[0], s0[1]), pack2(s0[2], s0[3]), pack2(s1[0], s1[1]), pack2(s1[2], s1[3])};
    *(u32x4*)(out + (size_t)(b * SQ + q) * 2048 + br * 1024 + ns * 128 + c8 * 16) = o;
  }
  if (w < 4) STAMP(1, 2);
}

extern "C" void kernel_launch(void* const* d_in, const int* in_sizes, int n_in, void* d_out, int out_size,
                              void* d_ws, size_t ws_size, hipStream_t stream) {
  const char* xA = (const char*)d_in[0]; const char* KA = (const char*)d_in[1]; const char* VA = (const char*)d_in[2];
  const char* wqA = (const char*)d_in[3]; const char* woA = (const char*)d_in[4];
  const char* xB = (const char*)d_in[5]; const char* KB = (const char*)d_in[6]; const char* VB = (const char*)d_in[7];
  const char* wqB = (const char*)d_in[8]; const char* woB = (const char*)d_in[9];
  u32x4* Obuf = (u32x4*)d_ws;
  (void)hipFuncSetAttribute((const void*)attn_kernel, hipFuncAttributeMaxDynamicSharedMemorySize, ATTN_LDS);
  (void)hipFuncSetAttribute((const void*)oproj_kernel, hipFuncAttributeMaxDynamicSharedMemorySize, 131072);
  attn_kernel<<<256, 256, ATTN_LDS, stream>>>(xA, KA, VA, wqA, xB, KB, VB, wqB, Obuf);
  oproj_kernel<<<256, 512, 131072, stream>>>(Obuf, woA, woB, (char*)d_out);
}
```

```cpp
#include <hip/hip_runtime.h>
#include <hip/hip_bf16.h>
#include <stdint.h>

typedef short s16x4 __attribute__((ext_vector_type(4)));
typedef float f32x4 __attribute__((ext_vector_type(4)));
typedef unsigned u32x4 __attribute__((ext_vector_type(4)));
typedef unsigned u32x2 __attribute__((ext_vector_type(2)));
typedef __bf16 bf16x8 __attribute__((ext_vector_type(8)));
typedef __bf16 bf16x2 __attribute__((ext_vector_type(2)));
typedef float f32x2 __attribute__((ext_vector_type(2)));
typedef __attribute__((address_space(3))) s16x4* lds_s16x4_p;
typedef __attribute__((address_space(3))) char* lds_char_p;

constexpr int NB = 16, SQ = 48, DM = 512, NH = 32, SK = 1024, DH = 128;
constexpr int STAGE = 49152;
constexpr int NSLOT = 3;
constexpr int NT = SK / 32;
constexpr int ATTN_LDS = NSLOT * STAGE + 16384;
constexpr float SCL2 = 0.08838834764831845f * 1.4426950408889634f;
constexpr float RESCALE_THR = 8.0f / SCL2;
#define DMA_ORDER 0

#define STAMP(k, i)

__device__ __forceinline__ unsigned pack2(float a, float b) {
  f32x2 v = {a, b};
  bf16x2 r = __builtin_convertvector(v, bf16x2);
  return __builtin_bit_cast(unsigned, r);
}
__device__ __forceinline__ f32x4 mfma16(u32x4 a, u32x4 b, f32x4 c) {
  return __builtin_amdgcn_mfma_f32_16x16x32_bf16(__builtin_bit_cast(bf16x8, a), __builtin_bit_cast(bf16x8, b), c, 0, 0, 0);
}
__device__ __forceinline__ __amdgpu_buffer_rsrc_t mkrsrc(const void* p, unsigned bytes) {
  return __builtin_amdgcn_make_buffer_rsrc((void*)p, 0, bytes, 0x00020000);
}
__device__ __forceinline__ const char* rfl_ptr(const char* p) {
  uint64_t v = (uint64_t)p;
  unsigned lo = __builtin_amdgcn_readfirstlane((unsigned)v), hi = __builtin_amdgcn_readfirstlane((unsigned)(v >> 32));
  return (const char*)(((uint64_t)hi << 32) | lo);
}
__device__ __forceinline__ u32x4 tr_frag(const char* p, int hi_off) {
  s16x4 v0 = __builtin_amdgcn_ds_read_tr16_b64_v4i16((lds_s16x4_p)(p));
  s16x4 v1 = __builtin_amdgcn_ds_read_tr16_b64_v4i16((lds_s16x4_p)(p + hi_off));
  u32x2 a0 = __builtin_bit_cast(u32x2, v0), a1 = __builtin_bit_cast(u32x2, v1);
  return u32x4{a0[0], a0[1], a1[0], a1[1]};
}

__device__ __forceinline__ void dma4(unsigned m0v, __amdgpu_buffer_rsrc_t r, unsigned soff,
                                     unsigned v0, unsigned v1, unsigned v2, unsigned v3) {
  asm volatile(
      "s_mov_b32 m0, %0\n\ts_nop 0\n\t"
      "buffer_load_dwordx4 %1, %5, %6 offen nt lds\n\t"
      "buffer_load_dwordx4 %2, %5, %6 offen offset:1024 nt lds\n\t"
      "buffer_load_dwordx4 %3, %5, %6 offen offset:2048 nt lds\n\t"
      "buffer_load_dwordx4 %4, %5, %6 offen offset:3072 nt lds"
      :: "s"(m0v), "v"(v0), "v"(v1), "v"(v2), "v"(v3), "s"(r), "s"(soff) : "memory");
}
__device__ __forceinline__ void scale_acc(f32x4& x, float alpha) {
  asm volatile("" : "+a"(x));
  x *= alpha;
  asm volatile("" : "+a"(x));
}

__global__ __launch_bounds__(256, 1) void attn_kernel(
    const char* __restrict__ xA, const char* __restrict__ KA, const char* __restrict__ VA, const char* __restrict__ wqA,
    const char* __restrict__ xB, const char* __restrict__ KB, const char* __restrict__ VB, const char* __restrict__ wqB,
    u32x4* __restrict__ Obuf) {
  extern __shared__ __attribute__((aligned(16))) char smem[];
  const int bid = blockIdx.x, xj = bid & 7, ci = bid >> 3;
  const int w = __builtin_amdgcn_readfirstlane(threadIdx.x >> 6), l = threadIdx.x & 63;
  const int r = l & 15, g = l >> 4;
  const unsigned ldsbase = (unsigned)(uintptr_t)(lds_char_p)smem;
  const int wid = bid * 4 + w; (void)wid;
  STAMP(0, 0);

  const int hM = 4 * xj + (ci & 3), bM = 2 * (ci >> 2);
  const int bG = ci >> 1, hG = 4 * xj + 2 * (ci & 1);
  const size_t U = (size_t)SK * DH * 2;

  __amdgpu_buffer_rsrc_t drs[3];
  unsigned dvo[3][4], dso[3], dm0[3];
#pragma unroll
  for (int e = 0; e < 3; e++) {
    const int G = 3 * w + e, st = G >> 2, isV = (G >> 1) & 1, hf = G & 1;
    const char* base = st == 2 ? (isV ? VA : KA) + (size_t)(bG * 8 + xj) * U
                               : (isV ? VB : KB) + (size_t)((bM + st) * NH + hM) * U;
    drs[e] = mkrsrc(rfl_ptr(base), (unsigned)U);
    dso[e] = hf * 4096;
    dm0[e] = ldsbase + G * 4096;
#pragma unroll
    for (int i = 0; i < 4; i++) {
      const int sw = isV ? ((4 * (i & 1) + g) << 1) : (4 * g + i);
      dvo[e][i] = g * 256 + ((r ^ sw) << 4);
    }
  }
  auto issue_group = [&](int e, int t, int slot) {
    dma4(dm0[e] + slot * STAGE, drs[e], dso[e] + t * 8192, dvo[e][0], dvo[e][1], dvo[e][2], dvo[e][3]);
  };
  if (DMA_ORDER == 1) {
#pragma unroll
    for (int kk = 0; kk < 9; kk++) issue_group(kk % 3, kk / 3, kk / 3);
  }

  const int tb = w < 2 ? bM + w : bG, th = w < 2 ? hM : hG + (w - 2);
  u32x4 qf[3][4];
  {
    const char* xp = rfl_ptr((w < 2 ? xB : xA) + (size_t)tb * SQ * DM * 2);
    const char* wp = rfl_ptr((w < 2 ? wqB : wqA) + (size_t)th * DH * 2);
    __amdgpu_buffer_rsrc_t rx = mkrsrc(xp, SQ * DM * 2);
    __amdgpu_buffer_rsrc_t rw = mkrsrc(wp, (DM - 1) * NH * DH * 2 + DH * 2);
    const int xvo = g * (DM * 2) + r * 16;
    const int wvo = g * 8 * (NH * DH * 2) + r * 16;
    char* xs = smem + NSLOT * STAGE + w * 4096;
    const int rb = w < 2 ? (ci >> 2) + (ci & 3) : bG + 2 * (ci & 1) + (w & 1);
    const int rs = w < 2 ? (ci >> 4) * 2 + w : (ci >> 3);
    constexpr int QD = 4;
    u32x4 wb[QD][8], xr[3][4], xf[2][3][4];
    f32x4 qa[8][3];
#pragma unroll
    for (int i = 0; i < 8; i++)
#pragma unroll
      for (int qb = 0; qb < 3; qb++) qa[i][qb] = f32x4{0.f, 0.f, 0.f, 0.f};
    auto ldw = [&](int kk) {
      const int ks = (((kk >> 2) + rb) & 3) * 4 + ((kk + rs) & 3);
#pragma unroll
      for (int j = 0; j < 8; j++)
        wb[kk % QD][j] = __builtin_amdgcn_raw_buffer_load_b128(rw, wvo, (ks * 32 + j) * (NH * DH * 2), 0);
    };
    auto ldx = [&](int B) {
#pragma unroll
      for (int qb = 0; qb < 3; qb++)
#pragma unroll
        for (int i = 0; i < 4; i++)
          xr[qb][i] = __builtin_amdgcn_raw_buffer_load_b128(rx, xvo, ((B + rb) & 3) * 256 + (qb * 16 + 4 * i) * (DM * 2), 0);
    };
    auto cvtx = [&](int B) {
#pragma unroll
      for (int qb = 0; qb < 3; qb++) {
#pragma unroll
        for (int i = 0; i < 4; i++) *(u32x4*)(xs + (4 * i + g) * 256 + ((r ^ (4 * i + g)) << 4)) = xr[qb][i];
#pragma unroll
        for (int i = 0; i < 4; i++) xf[B & 1][qb][i] = *(const u32x4*)(xs + r * 256 + (((4 * ((i + rs) & 3) + g) ^ r) << 4));
      }
    };
    ldx(0);
#pragma unroll
    for (int kk = 0; kk < QD - 1; kk++) ldw(kk);
    __builtin_amdgcn_sched_barrier(0);
    if (DMA_ORDER == 3) {
#pragma unroll
      for (int kk = 0; kk < 9; kk++) issue_group(kk % 3, kk / 3, kk / 3);
    }
    cvtx(0);
    __builtin_amdgcn_sched_barrier(0);
#pragma unroll
    for (int kk = 0; kk < 16; kk++) {
      if (kk + QD - 1 < 16) ldw(kk + QD - 1);
      if ((kk & 3) == 0 && kk < 12) ldx(kk / 4 + 1);
      if (DMA_ORDER == 0 && kk < 9) issue_group(kk % 3, kk / 3, kk / 3);
      if (DMA_ORDER == 4 && (kk == 0 || kk == 6 || kk == 12)) {
#pragma unroll
        for (int e = 0; e < 3; e++) issue_group(e, kk / 6, kk / 6);
      }
      if ((kk & 3) == 3 && kk < 12) cvtx(kk / 4 + 1);
      __builtin_amdgcn_sched_barrier(0);
#pragma unroll
      for (int c = 0; c < 4; c++) {
        u32x4 lo, hi;
#pragma unroll
        for (int p = 0; p < 4; p++) {
          lo[p] = __builtin_amdgcn_perm(wb[kk % QD][2 * p + 1][c], wb[kk % QD][2 * p][c], 0x05040100u);
          hi[p] = __builtin_amdgcn_perm(wb[kk % QD][2 * p + 1][c], wb[kk % QD][2 * p][c], 0x07060302u);
        }
#pragma unroll
        for (int qb = 0; qb < 3; qb++) {
          qa[2 * c][qb] = mfma16(lo, xf[(kk >> 2) & 1][qb][kk & 3], qa[2 * c][qb]);
          qa[2 * c + 1][qb] = mfma16(hi, xf[(kk >> 2) & 1][qb][kk & 3], qa[2 * c + 1][qb]);
        }
      }
      __builtin_amdgcn_sched_barrier(0);
    }
#pragma unroll
    for (int qb = 0; qb < 3; qb++)
#pragma unroll
      for (int s = 0; s < 4; s++)
#pragma unroll
        for (int p = 0; p < 4; p++) qf[qb][s][p] = pack2(qa[2 * p][qb][s], qa[2 * p + 1][qb][s]);
  }
  STAMP(0, 1);
  if (DMA_ORDER == 2) {
#pragma unroll
    for (int kk = 0; kk < 9; kk++) issue_group(kk % 3, kk / 3, kk / 3);
  }

  const int strm = w < 2 ? w : 2;
  unsigned kaddr[4], vaddr0;
#pragma unroll
  for (int s = 0; s < 4; s++) kaddr[s] = strm * 16384 + r * 256 + (((4 * g + s) ^ (4 * (r & 3) + (r >> 2))) << 4);
  {
    const int row = 4 * g + (r >> 2), p = r & 3;
    vaddr0 = strm * 16384 + 8192 + row * 256 + ((((p >> 1)) ^ ((row & 7) << 1)) << 4) + (p & 1) * 8;
  }
  f32x4 oa[8][3];
#pragma unroll
  for (int i = 0; i < 8; i++)
#pragma unroll
    for (int qb = 0; qb < 3; qb++) oa[i][qb] = f32x4{0.f, 0.f, 0.f, 0.f};
  float mrow[3] = {-INFINITY, -INFINITY, -INFINITY}, lsum[3] = {0.f, 0.f, 0.f};

  asm volatile("s_waitcnt vmcnt(24) lgkmcnt(0)\n\ts_barrier" ::: "memory");
  int slot = 0;
  for (int t = 0; t < NT; t++) {
    const bool refill = t >= 1 && t + 2 < NT;
    const int fs = slot == 0 ? 2 : slot - 1;
    const char* sb = smem + slot * STAGE;
    u32x4 kf[2][4], vf[8];
#pragma unroll
    for (int s = 0; s < 4; s++)
#pragma unroll
      for (int kb = 0; kb < 2; kb++) kf[kb][s] = *(const u32x4*)(sb + kaddr[s] + kb * 4096);
    if (refill) issue_group(0, t + 2, fs);
    __builtin_amdgcn_sched_barrier(0);
    f32x4 sa[2][3];
#pragma unroll
    for (int s = 0; s < 4; s++) {
#pragma unroll
      for (int kb = 0; kb < 2; kb++)
#pragma unroll
        for (int qb = 0; qb < 3; qb++)
          sa[kb][qb] = mfma16(kf[kb][s], qf[qb][s], s == 0 ? f32x4{0.f, 0.f, 0.f, 0.f} : sa[kb][qb]);
      vf[2 * s] = tr_frag(sb + (vaddr0 ^ ((2 * s) << 5)), 4096);
      vf[2 * s + 1] = tr_frag(sb + (vaddr0 ^ ((2 * s + 1) << 5)), 4096);
      __builtin_amdgcn_sched_barrier(0);
    }
    if (refill) issue_group(1, t + 2, fs);
    float mx[3];
    bool need = false;
#pragma unroll
    for (int qb = 0; qb < 3; qb++) {
      float m0 = __builtin_fmaxf(__builtin_fmaxf(sa[0][qb][0], sa[0][qb][1]), sa[0][qb][2]);
      m0 = __builtin_fmaxf(__builtin_fmaxf(m0, sa[0][qb][3]), sa[1][qb][0]);
      m0 = __builtin_fmaxf(__builtin_fmaxf(m0, sa[1][qb][1]), sa[1][qb][2]);
      mx[qb] = __builtin_fmaxf(m0, sa[1][qb][3]);
      need |= mx[qb] > mrow[qb] + RESCALE_THR;
    }
    if (__builtin_expect(__builtin_amdgcn_ballot_w64(need) != 0, 0)) {
#pragma unroll
      for (int qb = 0; qb < 3; qb++) {
        float m = mx[qb];
        m = fmaxf(m, __shfl_xor(m, 16));
        m = fmaxf(m, __shfl_xor(m, 32));
        m = fmaxf(m, mrow[qb]);
        const float alpha = __builtin_amdgcn_exp2f((mrow[qb] - m) * SCL2);
        mrow[qb] = m;
        lsum[qb] *= alpha;
#pragma unroll
        for (int i = 0; i < 8; i++) scale_acc(oa[i][qb], alpha);
      }
    }
    u32x4 pf[3];
#pragma unroll
    for (int qb = 0; qb < 3; qb++) {
      const float mc = mrow[qb] * SCL2;
      float p[8];
#pragma unroll
      for (int kb = 0; kb < 2; kb++)
#pragma unroll
        for (int e = 0; e < 4; e++) p[kb * 4 + e] = __builtin_amdgcn_exp2f(__builtin_fmaf(sa[kb][qb][e], SCL2, -mc));
      lsum[qb] += ((p[0] + p[1]) + (p[2] + p[3])) + ((p[4] + p[5]) + (p[6] + p[7]));
#pragma unroll
      for (int e = 0; e < 4; e++) pf[qb][e] = pack2(p[2 * e], p[2 * e + 1]);
    }
    if (refill) issue_group(2, t + 2, fs);
#pragma unroll
    for (int qb = 0; qb < 3; qb++)
#pragma unroll
      for (int db = 0; db < 8; db++) oa[db][qb] = mfma16(vf[db], pf[qb], oa[db][qb]);
    slot = slot == 2 ? 0 : slot + 1;
    if (t >= NT - 2) asm volatile("s_waitcnt vmcnt(0) lgkmcnt(0)\n\ts_barrier" ::: "memory");
    else asm volatile("s_waitcnt vmcnt(12) lgkmcnt(0)\n\ts_barrier" ::: "memory");
  }
  STAMP(0, 10);

  const int task = ((w < 2 ? 1 : 0) * NB + tb) * NH + th;
#pragma unroll
  for (int qb = 0; qb < 3; qb++) {
    float s = lsum[qb];
    s += __shfl_xor(s, 16);
    s += __shfl_xor(s, 32);
    const float inv = 1.0f / s;
#pragma unroll
    for (int u = 0; u < 4; u++) {
      u32x4 o;
      o[0] = pack2(oa[2 * u][qb][0] * inv, oa[2 * u][qb][1] * inv);
      o[1] = pack2(oa[2 * u][qb][2] * inv, oa[2 * u][qb][3] * inv);
      o[2] = pack2(oa[2 * u + 1][qb][0] * inv, oa[2 * u + 1][qb][1] * inv);
      o[3] = pack2(oa[2 * u + 1][qb][2] * inv, oa[2 * u + 1][qb][3] * inv);
      Obuf[((task * 4 + u) * 3 + qb) * 64 + l] = o;
    }
  }
  STAMP(0, 11);
}

__global__ __launch_bounds__(512, 2) void oproj_kernel(const u32x4* __restrict__ Obuf, const char* __restrict__ woA,
                                                       const char* __restrict__ woB, char* __restrict__ out) {
  extern __shared__ __attribute__((aligned(16))) char smem[];
  const int bid = blockIdx.x, xj = bid & 7, ci = bid >> 3;
  const int br = xj >> 2, b = 8 * ((xj >> 1) & 1) + (ci >> 2), ns = 4 * (xj & 1) + (ci & 3);
  const int w = __builtin_amdgcn_readfirstlane(threadIdx.x >> 6), l = threadIdx.x & 63;
  const int r = l & 15, g = l >> 4;
  const int wid = bid * 4 + (w & 3); (void)wid;
  if (w < 4) STAMP(1, 0);
  const unsigned ring = (unsigned)(uintptr_t)(lds_char_p)smem + w * 16384;
  const char* wp = rfl_ptr((br ? woB : woA) + (size_t)(w * 512) * (DM * 2) + ns * 128);
  __amdgpu_buffer_rsrc_t rw = mkrsrc(wp, 511 * DM * 2 + 128);
  const char* op = rfl_ptr((const char*)(Obuf + (size_t)((br * NB + b) * NH + w * 4) * 12 * 64));
  __amdgpu_buffer_rsrc_t ro = mkrsrc(op, 4 * 12 * 1024);
  unsigned wvo[4];
#pragma unroll
  for (int i = 0; i < 4; i++) wvo[i] = (8 * i + (l >> 3)) * (DM * 2) - 1024 * i + (((l & 7) ^ (((l >> 4) & 3) << 1)) << 4);
  const unsigned ovo = l * 16;
  constexpr int QD = 4;
  u32x4 ob[QD][3];
  f32x4 acc[4][3];
#pragma unroll
  for (int i = 0; i < 4; i++)
#pragma unroll
    for (int qb = 0; qb < 3; qb++) acc[i][qb] = f32x4{0.f, 0.f, 0.f, 0.f};
  const int rot = 4 * (ci & 3) + 2 * (ci >> 2);
  auto ldstep = [&](int kk) {
    const int ks = (kk + rot) & 15;
    asm volatile(
        "s_mov_b32 m0, %3\n\ts_nop 0\n\t"
        "buffer_load_dwordx4 %4, %8, %9 offen lds\n\t"
        "buffer_load_dwordx4 %5, %8, %9 offen offset:1024 lds\n\t"
        "buffer_load_dwordx4 %6, %8, %9 offen offset:2048 lds\n\t"
        "buffer_load_dwordx4 %7, %8, %9 offen offset:3072 lds\n\t"
        "buffer_load_dwordx4 %0, %10, %11, %12 offen\n\t"
        "buffer_load_dwordx4 %1, %10, %11, %12 offen offset:1024\n\t"
        "buffer_load_dwordx4 %2, %10, %11, %12 offen offset:2048"
        : "=&v"(ob[kk % QD][0]), "=&v"(ob[kk % QD][1]), "=&v"(ob[kk % QD][2])
        : "s"(ring + (kk % QD) * 4096), "v"(wvo[0]), "v"(wvo[1]), "v"(wvo[2]), "v"(wvo[3]), "s"(rw), "s"(ks * 32 * DM * 2),
          "v"(ovo), "s"(ro), "s"(ks * 3072)
        : "memory");
  };
  const int row = 4 * g + (r >> 2), p = r & 3;
  const unsigned aoff = w * 16384 + row * 128 + (((p >> 1) ^ (((row >> 1) & 3) << 1)) << 4) + (p & 1) * 8;
#pragma unroll
  for (int kk = 0; kk < QD; kk++) ldstep(kk);
#pragma unroll
  for (int kk = 0; kk < 16; kk++) {
    if (kk < 13) asm volatile("s_waitcnt vmcnt(21)" : "+v"(ob[kk % QD][0]), "+v"(ob[kk % QD][1]), "+v"(ob[kk % QD][2]) :: "memory");
    else if (kk == 13) asm volatile("s_waitcnt vmcnt(14)" : "+v"(ob[kk % QD][0]), "+v"(ob[kk % QD][1]), "+v"(ob[kk % QD][2]) :: "memory");
    else if (kk == 14) asm volatile("s_waitcnt vmcnt(7)" : "+v"(ob[kk % QD][0]), "+v"(ob[kk % QD][1]), "+v"(ob[kk % QD][2]) :: "memory");
    else asm volatile("s_waitcnt vmcnt(0)" : "+v"(ob[kk % QD][0]), "+v"(ob[kk % QD][1]), "+v"(ob[kk % QD][2]) :: "memory");
    u32x4 af[4];
#pragma unroll
    for (int db = 0; db < 4; db++) af[db] = tr_frag(smem + (kk % QD) * 4096 + (aoff ^ (db << 5)), 2048);
#pragma unroll
    for (int db = 0; db < 4; db++)
#pragma unroll
      for (int qb = 0; qb < 3; qb++) acc[db][qb] = mfma16(af[db], ob[kk % QD][qb], acc[db][qb]);
    __builtin_amdgcn_sched_barrier(0);
    if (kk + QD < 16) {
      asm volatile("s_waitcnt lgkmcnt(0)" ::: "memory");
      ldstep(kk + QD);
    }
  }
  if (w < 4) STAMP(1, 1);
  __syncthreads();
  f32x4* red = (f32x4*)smem;
#pragma unroll
  for (int qb = 0; qb < 3; qb++)
#pragma unroll
    for (int db = 0; db < 4; db++) red[((w * 3 + qb) * 4 + db) * 64 + l] = acc[db][qb];
  __syncthreads();
  if (w < 6) {
    const int q = 8 * w + (l >> 3), c8 = l & 7;
    const int qb = q >> 4, idx = (qb * 4 + (c8 >> 1)) * 64 + (2 * (c8 & 1)) * 16 + (q & 15);
    f32x4 s0 = {0.f, 0.f, 0.f, 0.f}, s1 = s0;
#pragma unroll
    for (int ww = 0; ww < 8; ww++) {
      s0 += red[ww * 768 + idx];
      s1 += red[ww * 768 + idx + 16];
    }
    u32x4 o = {pack2(s0[0], s0[1]), pack2(s0[2], s0[3]), pack2(s1[0], s1[1]), pack2(s1[2], s1[3])};
    *(u32x4*)(out + (size_t)(b * SQ + q) * 2048 + br * 1024 + ns * 128 + c8 * 16) = o;
  }
  if (w < 4) STAMP(1, 2);
}

extern "C" void kernel_launch(void* const* d_in, const int* in_sizes, int n_in, void* d_out, int out_size,
                              void* d_ws, size_t ws_size, hipStream_t stream) {
  const char* xA = (const char*)d_in[0]; const char* KA = (const char*)d_in[1]; const char* VA = (const char*)d_in[2];
  const char* wqA = (const char*)d_in[3]; const char* woA = (const char*)d_in[4];
  const char* xB = (const char*)d_in[5]; const char* KB = (const char*)d_in[6]; const char* VB = (const char*)d_in[7];
  const char* wqB = (const char*)d_in[8]; const char* woB = (const char*)d_in[9];
  u32x4* Obuf = (u32x4*)d_ws;
  (void)hipFuncSetAttribute((const void*)attn_kernel, hipFuncAttributeMaxDynamicSharedMemorySize, ATTN_LDS);
  (void)hipFuncSetAttribute((const void*)oproj_kernel, hipFuncAttributeMaxDynamicSharedMemorySize, 131072);
  attn_kernel<<<256, 256, ATTN_LDS, stream>>>(xA, KA, VA, wqA, xB, KB, VB, wqB, Obuf);
  oproj_kernel<<<256, 512, 131072, stream>>>(Obuf, woA, woB, (char*)d_out);
}
```

```cpp
#include <hip/hip_runtime.h>
#include <hip/hip_bf16.h>
#include <stdint.h>

typedef short s16x4 __attribute__((ext_vector_type(4)));
typedef float f32x4 __attribute__((ext_vector_type(4)));
typedef unsigned u32x4 __attribute__((ext_vector_type(4)));
typedef unsigned u32x2 __attribute__((ext_vector_type(2)));
typedef __bf16 bf16x8 __attribute__((ext_vector_type(8)));
typedef __bf16 bf16x2 __attribute__((ext_vector_type(2)));
typedef float f32x2 __attribute__((ext_vector_type(2)));
typedef __attribute__((address_space(3))) s16x4* lds_s16x4_p;
typedef __attribute__((address_space(3))) char* lds_char_p;

constexpr int NB = 16, SQ = 48, DM = 512, NH = 32, SK = 1024, DH = 128;
constexpr int STAGE = 49152;
constexpr int NSLOT = 3;
constexpr int NT = SK / 32;
constexpr int ATTN_LDS = NSLOT * STAGE + 16384;
constexpr float SCL2 = 0.08838834764831845f * 1.4426950408889634f;
constexpr float RESCALE_THR = 8.0f / SCL2;
constexpr int QW_SLOT = 24576, QX_SLOT = 18432;
#define KV0_STEP 13
constexpr int QNW = 4, QNX = 3;
constexpr int QW_OFF = ATTN_LDS - QNW * QW_SLOT - QNX * QX_SLOT, QX_OFF = ATTN_LDS - QNX * QX_SLOT;
#define MAPV 1
#define NSMAP (2 * (ci & 3) + (xj & 1))
#define OROT 0
#define WO_TILE 22
#define QROT (ci >> 2)

#define STAMP(k, i)

__device__ __forceinline__ unsigned pack2(float a, float b) {
  f32x2 v = {a, b};
  bf16x2 r = __builtin_convertvector(v, bf16x2);
  return __builtin_bit_cast(unsigned, r);
}
__device__ __forceinline__ f32x4 mfma16(u32x4 a, u32x4 b, f32x4 c) {
  return __builtin_amdgcn_mfma_f32_16x16x32_bf16(__builtin_bit_cast(bf16x8, a), __builtin_bit_cast(bf16x8, b), c, 0, 0, 0);
}
__device__ __forceinline__ __amdgpu_buffer_rsrc_t mkrsrc(const void* p, unsigned bytes) {
  return __builtin_amdgcn_make_buffer_rsrc((void*)p, 0, bytes, 0x00020000);
}
__device__ __forceinline__ const char* rfl_ptr(const char* p) {
  uint64_t v = (uint64_t)p;
  unsigned lo = __builtin_amdgcn_readfirstlane((unsigned)v), hi = __builtin_amdgcn_readfirstlane((unsigned)(v >> 32));
  return (const char*)(((uint64_t)hi << 32) | lo);
}
__device__ __forceinline__ u32x4 tr_frag(const char* p, int hi_off) {
  s16x4 v0 = __builtin_amdgcn_ds_read_tr16_b64_v4i16((lds_s16x4_p)(p));
  s16x4 v1 = __builtin_amdgcn_ds_read_tr16_b64_v4i16((lds_s16x4_p)(p + hi_off));
  u32x2 a0 = __builtin_bit_cast(u32x2, v0), a1 = __builtin_bit_cast(u32x2, v1);
  return u32x4{a0[0], a0[1], a1[0], a1[1]};
}

__device__ __forceinline__ void dma4(unsigned m0v, __amdgpu_buffer_rsrc_t r, unsigned soff,
                                     unsigned v0, unsigned v1, unsigned v2, unsigned v3) {
  asm volatile(
      "s_mov_b32 m0, %0\n\ts_nop 0\n\t"
      "buffer_load_dwordx4 %1, %5, %6 offen nt lds\n\t"
      "buffer_load_dwordx4 %2, %5, %6 offen offset:1024 nt lds\n\t"
      "buffer_load_dwordx4 %3, %5, %6 offen offset:2048 nt lds\n\t"
      "buffer_load_dwordx4 %4, %5, %6 offen offset:3072 nt lds"
      :: "s"(m0v), "v"(v0), "v"(v1), "v"(v2), "v"(v3), "s"(r), "s"(soff) : "memory");
}
__device__ __forceinline__ void dma2(unsigned m0v, __amdgpu_buffer_rsrc_t r, unsigned soff, unsigned v0, unsigned v1) {
  asm volatile(
      "s_mov_b32 m0, %0\n\ts_nop 0\n\t"
      "buffer_load_dwordx4 %1, %3, %4 offen lds\n\t"
      "buffer_load_dwordx4 %2, %3, %4 offen offset:1024 lds"
      :: "s"(m0v), "v"(v0), "v"(v1), "s"(r), "s"(soff) : "memory");
}
template <int N> __device__ __forceinline__ void wait_vm() {
  asm volatile("s_waitcnt vmcnt(%0) lgkmcnt(0)" :: "n"(N) : "memory");
}
template <int V> struct IC { static constexpr int value = V; };
template <class F> __device__ __forceinline__ void static_for16(F&& f) {
  f(IC<0>{}); f(IC<1>{}); f(IC<2>{}); f(IC<3>{}); f(IC<4>{}); f(IC<5>{}); f(IC<6>{}); f(IC<7>{});
  f(IC<8>{}); f(IC<9>{}); f(IC<10>{}); f(IC<11>{}); f(IC<12>{}); f(IC<13>{}); f(IC<14>{}); f(IC<15>{});
}
constexpr int qslot(int kk) { return kk % QNW; }
constexpr int qwait(int kk, int nx) {
  if (kk >= 15) return 63;
  int n = 0, wend[16] = {}, xend[8] = {};
  for (int i = 0; i < QNW; i++) {
    if (i != 1) { n += nx; xend[(i + 1) / 2] = n; }
    n += 6; wend[i] = n;
  }
  for (int k = 0; k < kk; k++) {
    if (k + QNW < 16) { n += 6; wend[k + QNW] = n; }
    if ((k & 1) && k < 11) { n += nx; xend[(k + 5) / 2] = n; }
    if (k == KV0_STEP) n += 12;
  }
  const int xe = nx ? xend[(kk + 1) / 2] : 0;
  const int need = wend[kk + 1] > xe ? wend[kk + 1] : xe;
  return n - need;
}
__device__ __forceinline__ void scale_acc(f32x4& x, float alpha) {
  asm volatile("" : "+a"(x));
  x *= alpha;
  asm volatile("" : "+a"(x));
}

__global__ __launch_bounds__(256, 1) void attn_kernel(
    const char* __restrict__ xA, const char* __restrict__ KA, const char* __restrict__ VA, const char* __restrict__ wqA,
    const char* __restrict__ xB, const char* __restrict__ KB, const char* __restrict__ VB, const char* __restrict__ wqB,
    const char* __restrict__ woA, const char* __restrict__ woB, u32x4* __restrict__ Obuf) {
  extern __shared__ __attribute__((aligned(16))) char smem[];
  const int bid = blockIdx.x, xj = bid & 7, ci = bid >> 3;
  const int w = __builtin_amdgcn_readfirstlane(threadIdx.x >> 6), l = threadIdx.x & 63;
  const int r = l & 15, g = l >> 4;
  const unsigned ldsbase = (unsigned)(uintptr_t)(lds_char_p)smem;
  const int wid = bid * 4 + w; (void)wid;
  STAMP(0, 0);

  const int hM = 4 * xj + (ci & 3), bM = 2 * (ci >> 2);
  const int bG = ci >> 1, kvh = xj;
  const int hG = 4 * kvh + 2 * (ci & 1);
  const size_t U = (size_t)SK * DH * 2;

  __amdgpu_buffer_rsrc_t drs[3];
  unsigned dvo[3][4], dso[3], dm0[3];
#pragma unroll
  for (int e = 0; e < 3; e++) {
    const int G = 3 * w + e, st = G >> 2, isV = (G >> 1) & 1, hf = G & 1;
    const char* base = st == 2 ? (isV ? VA : KA) + (size_t)(bG * 8 + kvh) * U
                               : (isV ? VB : KB) + (size_t)((bM + st) * NH + hM) * U;
    drs[e] = mkrsrc(rfl_ptr(base), (unsigned)U);
    dso[e] = hf * 4096;
    dm0[e] = ldsbase + G * 4096;
#pragma unroll
    for (int i = 0; i < 4; i++) {
      const int sw = isV ? ((4 * (i & 1) + g) << 1) : (4 * g + i);
      dvo[e][i] = g * 256 + ((r ^ sw) << 4);
    }
  }
  auto issue_group = [&](int e, int t, int slot) {
    dma4(dm0[e] + slot * STAGE, drs[e], dso[e] + t * 8192, dvo[e][0], dvo[e][1], dvo[e][2], dvo[e][3]);
  };
  const int tb = w < 2 ? bM + w : bG, th = w < 2 ? hM : hG + (w - 2);
  u32x4 qf[3][4];
  {
    __amdgpu_buffer_rsrc_t rwM = mkrsrc(rfl_ptr(wqB + (size_t)hM * DH * 2), (DM - 1) * NH * DH * 2 + DH * 2);
    __amdgpu_buffer_rsrc_t rwG = mkrsrc(rfl_ptr(wqA + (size_t)hG * DH * 2), (DM - 1) * NH * DH * 2 + 2 * DH * 2);
    const unsigned wv0 = (8 * w + g) * (NH * DH * 2) + ((r ^ (g | ((w & 1) << 3))) << 4);
    const unsigned wv1 = wv0 + 4 * (NH * DH * 2) - 1024;
    __amdgpu_buffer_rsrc_t rx = mkrsrc(rfl_ptr((w < 2 ? xB : xA) + (size_t)tb * SQ * DM * 2), SQ * DM * 2);
    const unsigned xv0 = (l >> 3) * (DM * 2) + (((l & 7) ^ g) << 4);
    const unsigned xv1 = (l >> 3) * (DM * 2) + (((l & 7) ^ (4 + g)) << 4) + 8 * (DM * 2) - 1024;
    const int rot = 2 * QROT;
    auto ldw = [&](int kk, int i) {
      const unsigned so = ((kk + rot) & 15) * 32 * (NH * DH * 2), m = ldsbase + QW_OFF + qslot(kk) * QW_SLOT + w * 2048;
      dma2(m + i * 8192, i == 0 ? rwM : rwG, so + (i == 2 ? DH * 2 : 0), wv0, wv1);
    };
    auto ldx = [&](int j, int i) {
      const unsigned so = ((2 * j + rot) & 15) * 64, m = ldsbase + QX_OFF + (j % QNX) * QX_SLOT + w * 6144;
      if (w < 3) dma2(m + i * 2048, rx, so + i * 16 * (DM * 2), xv0, xv1);
    };
#pragma unroll
    for (int k = 0; k < QNW; k++) {
#pragma unroll
      for (int i = 0; i < 3; i++) if (k != 1) ldx((k + 1) / 2, i);
#pragma unroll
      for (int i = 0; i < 3; i++) ldw(k, i);
    }
    f32x4 qa[8][3];
#pragma unroll
    for (int i = 0; i < 8; i++)
#pragma unroll
      for (int qb = 0; qb < 3; qb++) qa[i][qb] = f32x4{0.f, 0.f, 0.f, 0.f};
    const unsigned abase = QW_OFF + (w < 2 ? 0 : w - 1) * 8192 + (8 * g + (r >> 2)) * 256 + ((r & 2) << 2);
    const unsigned asw = (r >> 2) | ((g & 1) << 3), ach = 4 * (r & 3);
    const unsigned xbase = QX_OFF + (w < 2 ? w : 2) * 6144 + r * 128 + ((g ^ (r >> 1)) << 4);
    u32x4 af[2][8], xf[2][3];
    auto rdx = [&](int kk, int qb) {
      xf[kk & 1][qb] = *(const u32x4*)(smem + ((kk >> 1) % QNX) * QX_SLOT + ((xbase + qb * 2048) ^ ((kk & 1) << 6)));
    };
    auto rda = [&](int kk, int i) {
      af[kk & 1][i] = tr_frag(smem + qslot(kk) * QW_SLOT + ((abase + (((ach + (i >> 1)) ^ asw) << 4)) ^ ((i & 1) << 3)), 1024);
    };
    if (w < 3) wait_vm<qwait(-1, 6)>(); else wait_vm<qwait(-1, 0)>();
    asm volatile("s_barrier" ::: "memory");
#pragma unroll
    for (int qb = 0; qb < 3; qb++) rdx(0, qb);
#pragma unroll
    for (int i = 0; i < 8; i++) rda(0, i);
    static_for16([&](auto kc) {
      constexpr int kk = decltype(kc)::value;
      if (w < 3) wait_vm<qwait(kk, 6)>(); else wait_vm<qwait(kk, 0)>();
      asm volatile("s_barrier" ::: "memory");
#pragma unroll
      for (int i = 0; i < 8; i++) {
        if (kk < 15) {
          if (i < 3) rdx(kk + 1, i);
          rda(kk + 1, i);
        }
#pragma unroll
        for (int qb = 0; qb < 3; qb++) qa[i][qb] = mfma16(af[kk & 1][i], xf[kk & 1][qb], qa[i][qb]);
        if (kk + QNW < 16 && i < 3) ldw(kk + QNW, i);
        if ((kk & 1) && kk < 11 && i >= 3 && i < 6) ldx((kk + 5) / 2, i - 3);
        if (kk == KV0_STEP && i < 3) issue_group(i, 0, 0);
        if (kk == 15 && i >= 5) issue_group(i - 5, 1, 1);
        __builtin_amdgcn_sched_barrier(0);
      }
    });
    const bool odd = g >> 1;
#pragma unroll
    for (int qb = 0; qb < 3; qb++)
#pragma unroll
      for (int s = 0; s < 4; s++) {
        const unsigned a0 = pack2(qa[2 * s][qb][0], qa[2 * s][qb][1]), a1 = pack2(qa[2 * s][qb][2], qa[2 * s][qb][3]);
        const unsigned b0 = pack2(qa[2 * s + 1][qb][0], qa[2 * s + 1][qb][1]), b1 = pack2(qa[2 * s + 1][qb][2], qa[2 * s + 1][qb][3]);
        qf[qb][s] = u32x4{odd ? b0 : a0, odd ? b1 : a1, odd ? a0 : b0, odd ? a1 : b1};
      }
  }
#pragma unroll
  for (int e = 0; e < 3; e++) issue_group(e, 2, 2);
  STAMP(0, 1);

  const int strm = w < 2 ? w : 2;
  unsigned kaddr[4], vaddr0;
#pragma unroll
  for (int s = 0; s < 4; s++) kaddr[s] = strm * 16384 + r * 256 + (((4 * g + s) ^ (4 * (r & 3) + (r >> 2))) << 4);
  {
    const int row = 4 * g + (r >> 2), p = r & 3;
    vaddr0 = strm * 16384 + 8192 + row * 256 + ((((p >> 1)) ^ ((row & 7) << 1)) << 4) + (p & 1) * 8;
  }
  f32x4 oa[8][3];
#pragma unroll
  for (int i = 0; i < 8; i++)
#pragma unroll
    for (int qb = 0; qb < 3; qb++) oa[i][qb] = f32x4{0.f, 0.f, 0.f, 0.f};
  float mrow[3] = {-INFINITY, -INFINITY, -INFINITY}, lsum[3] = {0.f, 0.f, 0.f};

  asm volatile("s_waitcnt vmcnt(24) lgkmcnt(0)\n\ts_barrier" ::: "memory");
  int slot = 0;
  for (int t = 0; t < NT; t++) {
    const bool refill = t >= 1 && t + 2 < NT;
    const int fs = slot == 0 ? 2 : slot - 1;
    const char* sb = smem + slot * STAGE;
    u32x4 kf[2][4], vf[8];
#pragma unroll
    for (int s = 0; s < 4; s++)
#pragma unroll
      for (int kb = 0; kb < 2; kb++) kf[kb][s] = *(const u32x4*)(sb + kaddr[s] + kb * 4096);
    if (refill) issue_group(0, t + 2, fs);
    __builtin_amdgcn_sched_barrier(0);
    f32x4 sa[2][3];
#pragma unroll
    for (int s = 0; s < 4; s++) {
#pragma unroll
      for (int kb = 0; kb < 2; kb++)
#pragma unroll
        for (int qb = 0; qb < 3; qb++)
          sa[kb][qb] = mfma16(kf[kb][s], qf[qb][s], s == 0 ? f32x4{0.f, 0.f, 0.f, 0.f} : sa[kb][qb]);
      vf[2 * s] = tr_frag(sb + (vaddr0 ^ ((2 * s) << 5)), 4096);
      vf[2 * s + 1] = tr_frag(sb + (vaddr0 ^ ((2 * s + 1) << 5)), 4096);
      __builtin_amdgcn_sched_barrier(0);
    }
    if (refill) issue_group(1, t + 2, fs);
    if (t == WO_TILE) {
      __amdgpu_buffer_rsrc_t rp = mkrsrc(rfl_ptr((bid & 128 ? woB : woA) + (size_t)((bid & 127) * 4 + w) * 8192), 8192);
      asm volatile("s_mov_b32 m0, %0\n\ts_nop 0\n\tbuffer_load_dword %1, %2, 0 offen lds"
                   :: "s"(ldsbase + NSLOT * STAGE + w * 256), "v"(l * 128), "s"(rp) : "memory");
    }
    float mx[3];
    bool need = false;
#pragma unroll
    for (int qb = 0; qb < 3; qb++) {
      float m0 = __builtin_fmaxf(__builtin_fmaxf(sa[0][qb][0], sa[0][qb][1]), sa[0][qb][2]);
      m0 = __builtin_fmaxf(__builtin_fmaxf(m0, sa[0][qb][3]), sa[1][qb][0]);
      m0 = __builtin_fmaxf(__builtin_fmaxf(m0, sa[1][qb][1]), sa[1][qb][2]);
      mx[qb] = __builtin_fmaxf(m0, sa[1][qb][3]);
      need |= mx[qb] > mrow[qb] + RESCALE_THR;
    }
    if (__builtin_expect(__builtin_amdgcn_ballot_w64(need) != 0, 0)) {
#pragma unroll
      for (int qb = 0; qb < 3; qb++) {
        float m = mx[qb];
        m = fmaxf(m, __shfl_xor(m, 16));
        m = fmaxf(m, __shfl_xor(m, 32));
        m = fmaxf(m, mrow[qb]);
        const float alpha = __builtin_amdgcn_exp2f((mrow[qb] - m) * SCL2);
        mrow[qb] = m;
        lsum[qb] *= alpha;
#pragma unroll
        for (int i = 0; i < 8; i++) scale_acc(oa[i][qb], alpha);
      }
    }
    u32x4 pf[3];
#pragma unroll
    for (int qb = 0; qb < 3; qb++) {
      const float mc = mrow[qb] * SCL2;
      float p[8];
#pragma unroll
      for (int kb = 0; kb < 2; kb++)
#pragma unroll
        for (int e = 0; e < 4; e++) p[kb * 4 + e] = __builtin_amdgcn_exp2f(__builtin_fmaf(sa[kb][qb][e], SCL2, -mc));
      lsum[qb] += ((p[0] + p[1]) + (p[2] + p[3])) + ((p[4] + p[5]) + (p[6] + p[7]));
#pragma unroll
      for (int e = 0; e < 4; e++) pf[qb][e] = pack2(p[2 * e], p[2 * e + 1]);
    }
    if (refill) issue_group(2, t + 2, fs);
#pragma unroll
    for (int qb = 0; qb < 3; qb++)
#pragma unroll
      for (int db = 0; db < 8; db++) oa[db][qb] = mfma16(vf[db], pf[qb], oa[db][qb]);
    slot = slot == 2 ? 0 : slot + 1;
    if (t >= NT - 2) asm volatile("s_waitcnt vmcnt(0) lgkmcnt(0)\n\ts_barrier" ::: "memory");
    else asm volatile("s_waitcnt vmcnt(12) lgkmcnt(0)\n\ts_barrier" ::: "memory");
  }
  STAMP(0, 10);

  const int task = ((w < 2 ? 1 : 0) * NB + tb) * NH + th;
#pragma unroll
  for (int qb = 0; qb < 3; qb++) {
    float s = lsum[qb];
    s += __shfl_xor(s, 16);
    s += __shfl_xor(s, 32);
    const float inv = 1.0f / s;
#pragma unroll
    for (int u = 0; u < 4; u++) {
      u32x4 o;
      o[0] = pack2(oa[2 * u][qb][0] * inv, oa[2 * u][qb][1] * inv);
      o[1] = pack2(oa[2 * u][qb][2] * inv, oa[2 * u][qb][3] * inv);
      o[2] = pack2(oa[2 * u + 1][qb][0] * inv, oa[2 * u + 1][qb][1] * inv);
      o[3] = pack2(oa[2 * u + 1][qb][2] * inv, oa[2 * u + 1][qb][3] * inv);
      Obuf[((task * 4 + u) * 3 + qb) * 64 + l] = o;
    }
  }
  STAMP(0, 11);
}

constexpr int OQD = 8;
constexpr int OP_LDS = 4 * OQD * 4096;
__global__ __launch_bounds__(256, 1) void oproj_kernel(const u32x4* __restrict__ Obuf, const char* __restrict__ woA,
                                                       const char* __restrict__ woB, char* __restrict__ out) {
  extern __shared__ __attribute__((aligned(16))) char smem[];
  const int bid = blockIdx.x, xj = bid & 7, ci = bid >> 3;
  const int br = xj >> 2, b = 8 * ((xj >> 1) & 1) + (ci >> 2), ns = NSMAP;
  const int w = __builtin_amdgcn_readfirstlane(threadIdx.x >> 6), l = threadIdx.x & 63;
  const int r = l & 15, g = l >> 4;
  const int wid = bid * 4 + w; (void)wid;
  STAMP(1, 0);
  const unsigned ring = (unsigned)(uintptr_t)(lds_char_p)smem + w * (OQD * 4096);
  const char* wp = rfl_ptr((br ? woB : woA) + (size_t)(w * 1024) * (DM * 2) + ns * 128);
  __amdgpu_buffer_rsrc_t rw = mkrsrc(wp, 1023 * DM * 2 + 128);
  const char* op = rfl_ptr((const char*)(Obuf + (size_t)((br * NB + b) * NH + w * 8) * 12 * 64));
  __amdgpu_buffer_rsrc_t ro = mkrsrc(op, 8 * 12 * 1024);
  unsigned wvo[4];
#pragma unroll
  for (int i = 0; i < 4; i++) wvo[i] = (8 * i + (l >> 3)) * (DM * 2) - 1024 * i + (((l & 7) ^ (((l >> 4) & 3) << 1)) << 4);
  const unsigned ovo = l * 16;
  u32x4 ob[OQD][3];
  f32x4 acc[4][3];
#pragma unroll
  for (int i = 0; i < 4; i++)
#pragma unroll
    for (int qb = 0; qb < 3; qb++) acc[i][qb] = f32x4{0.f, 0.f, 0.f, 0.f};
  const int rot = OROT;
  auto ldw = [&](int kk, int sl) {
    asm volatile(
        "s_mov_b32 m0, %0\n\ts_nop 0\n\t"
        "buffer_load_dwordx4 %1, %5, %6 offen lds\n\t"
        "buffer_load_dwordx4 %2, %5, %6 offen offset:1024 lds\n\t"
        "buffer_load_dwordx4 %3, %5, %6 offen offset:2048 lds\n\t"
        "buffer_load_dwordx4 %4, %5, %6 offen offset:3072 lds"
        :: "s"(ring + sl * 4096), "v"(wvo[0]), "v"(wvo[1]), "v"(wvo[2]), "v"(wvo[3]), "s"(rw),
           "s"(((kk + rot) & 31) * 32 * DM * 2)
        : "memory");
  };
  auto ldo = [&](int kk, int sl) {
    asm volatile(
        "buffer_load_dwordx4 %0, %3, %4, %5 offen\n\t"
        "buffer_load_dwordx4 %1, %3, %4, %5 offen offset:1024\n\t"
        "buffer_load_dwordx4 %2, %3, %4, %5 offen offset:2048"
        : "=&v"(ob[sl][0]), "=&v"(ob[sl][1]), "=&v"(ob[sl][2])
        : "v"(ovo), "s"(ro), "s"(((kk + rot) & 31) * 3072)
        : "memory");
  };
  const int row = 4 * g + (r >> 2), p = r & 3;
  const unsigned aoff = w * (OQD * 4096) + row * 128 + (((p >> 1) ^ (((row >> 1) & 3) << 1)) << 4) + (p & 1) * 8;
#pragma unroll
  for (int kk = 0; kk < OQD; kk++) { ldw(kk, kk); ldo(kk, kk); }
  u32x4 af[2][4];
  asm volatile("s_waitcnt vmcnt(%0)" :: "n"(7 * OQD - 4) : "memory");
#pragma unroll
  for (int db = 0; db < 4; db++) af[0][db] = tr_frag(smem + (aoff ^ (db << 5)), 2048);
  auto step = [&](auto kc) {
    constexpr int kk = decltype(kc)::value, sl = kk % OQD;
    constexpr int last = kk + OQD - 1 < 31 ? kk + OQD - 1 : 31;
    constexpr int allow = kk < 31 ? 7 * (last - kk - 1) + 3 : 0;
    asm volatile("s_waitcnt vmcnt(%3) lgkmcnt(0)" : "+v"(ob[sl][0]), "+v"(ob[sl][1]), "+v"(ob[sl][2]) : "n"(allow) : "memory");
    if (kk < 31) {
#pragma unroll
      for (int db = 0; db < 4; db++) af[(kk + 1) & 1][db] = tr_frag(smem + ((kk + 1) % OQD) * 4096 + (aoff ^ (db << 5)), 2048);
    }
#pragma unroll
    for (int db = 0; db < 2; db++)
#pragma unroll
      for (int qb = 0; qb < 3; qb++) acc[db][qb] = mfma16(af[kk & 1][db], ob[sl][qb], acc[db][qb]);
    __builtin_amdgcn_sched_barrier(0);
    if (kk + OQD < 32) ldw(kk + OQD, sl);
#pragma unroll
    for (int db = 2; db < 4; db++)
#pragma unroll
      for (int qb = 0; qb < 3; qb++) acc[db][qb] = mfma16(af[kk & 1][db], ob[sl][qb], acc[db][qb]);
    __builtin_amdgcn_sched_barrier(0);
    if (kk + OQD < 32) ldo(kk + OQD, sl);
  };
  static_for16(step);
  STAMP(1, 3);
  static_for16([&](auto kc) { step(IC<16 + decltype(kc)::value>{}); });
  STAMP(1, 1);
  f32x4* red = (f32x4*)smem;
#pragma unroll
  for (int qb = 0; qb < 3; qb++)
#pragma unroll
    for (int db = 0; db < 4; db++) red[w * (OQD * 256) + (qb * 4 + db) * 64 + l] = acc[db][qb];
  __syncthreads();
#pragma unroll
  for (int it = 0; it < 2; it++) {
    const int wv = w + 4 * it;
    if (wv < 6) {
      const int q = 8 * wv + (l >> 3), c8 = l & 7;
      const int qb = q >> 4, idx = (qb * 4 + (c8 >> 1)) * 64 + (2 * (c8 & 1)) * 16 + (q & 15);
      f32x4 s0 = {0.f, 0.f, 0.f, 0.f}, s1 = s0;
#pragma unroll
      for (int ww = 0; ww < 4; ww++) {
        s0 += red[ww * (OQD * 256) + idx];
        s1 += red[ww * (OQD * 256) + idx + 16];
      }
      u32x4 o = {pack2(s0[0], s0[1]), pack2(s0[2], s0[3]), pack2(s1[0], s1[1]), pack2(s1[2], s1[3])};
      *(u32x4*)(out + (size_t)(b * SQ + q) * 2048 + br * 1024 + ns * 128 + c8 * 16) = o;
    }
  }
  STAMP(1, 2);
}

extern "C" void kernel_launch(void* const* d_in, const int* in_sizes, int n_in, void* d_out, int out_size,
                              void* d_ws, size_t ws_size, hipStream_t stream) {
  const char* xA = (const char*)d_in[0]; const char* KA = (const char*)d_in[1]; const char* VA = (const char*)d_in[2];
  const char* wqA = (const char*)d_in[3]; const char* woA = (const char*)d_in[4];
  const char* xB = (const char*)d_in[5]; const char* KB = (const char*)d_in[6]; const char* VB = (const char*)d_in[7];
  const char* wqB = (const char*)d_in[8]; const char* woB = (const char*)d_in[9];
  u32x4* Obuf = (u32x4*)d_ws;
  (void)hipFuncSetAttribute((const void*)attn_kernel, hipFuncAttributeMaxDynamicSharedMemorySize, ATTN_LDS);
  (void)hipFuncSetAttribute((const void*)oproj_kernel, hipFuncAttributeMaxDynamicSharedMemorySize, OP_LDS);
  attn_kernel<<<256, 256, ATTN_LDS, stream>>>(xA, KA, VA, wqA, xB, KB, VB, wqB, woA, woB, Obuf);
  oproj_kernel<<<256, 256, OP_LDS, stream>>>(Obuf, woA, woB, (char*)d_out);
}
```

```cpp
#include <hip/hip_runtime.h>
#include <hip/hip_bf16.h>
#include <stdint.h>

typedef short s16x4 __attribute__((ext_vector_type(4)));
typedef float f32x4 __attribute__((ext_vector_type(4)));
typedef unsigned u32x4 __attribute__((ext_vector_type(4)));
typedef unsigned u32x2 __attribute__((ext_vector_type(2)));
typedef __bf16 bf16x8 __attribute__((ext_vector_type(8)));
typedef __bf16 bf16x2 __attribute__((ext_vector_type(2)));
typedef float f32x2 __attribute__((ext_vector_type(2)));
typedef __attribute__((address_space(3))) s16x4* lds_s16x4_p;
typedef __attribute__((address_space(3))) char* lds_char_p;

constexpr int NB = 16, SQ = 48, DM = 512, NH = 32, SK = 1024, DH = 128;
constexpr int STAGE = 49152;
constexpr int NSLOT = 3;
constexpr int NT = SK / 32;
constexpr int ATTN_LDS = NSLOT * STAGE + 16384;
constexpr float SCL2 = 0.08838834764831845f * 1.4426950408889634f;
constexpr float RESCALE_THR = 8.0f / SCL2;
constexpr int QW_SLOT = 24576, QX_SLOT = 18432;
#define KV0_STEP 13
constexpr int QNW = 4, QNX = 3;
#define PFN 0
#define PFP 0
#define PFS0 2
#define PFDS 3
#define PFT0 5
#define PFDT 7
#define PFPOL " nt"
constexpr int pf_at(int kk) { int n = 0; for (int j = PFP; j < PFN; j++) if (PFS0 + (j - PFP) * PFDS == kk) n = j; return n; }
constexpr int QW_OFF = ATTN_LDS - QNW * QW_SLOT - QNX * QX_SLOT, QX_OFF = ATTN_LDS - QNX * QX_SLOT;
#define MAPV 1
#define NSMAP (2 * (ci & 3) + (xj & 1))
#define OROT 0
#define WO_TILE 26
#define QROT (ci >> 2)

#define STAMP(k, i)

__device__ __forceinline__ unsigned pack2(float a, float b) {
  f32x2 v = {a, b};
  bf16x2 r = __builtin_convertvector(v, bf16x2);
  return __builtin_bit_cast(unsigned, r);
}
__device__ __forceinline__ f32x4 mfma16(u32x4 a, u32x4 b, f32x4 c) {
  return __builtin_amdgcn_mfma_f32_16x16x32_bf16(__builtin_bit_cast(bf16x8, a), __builtin_bit_cast(bf16x8, b), c, 0, 0, 0);
}
__device__ __forceinline__ __amdgpu_buffer_rsrc_t mkrsrc(const void* p, unsigned bytes) {
  return __builtin_amdgcn_make_buffer_rsrc((void*)p, 0, bytes, 0x00020000);
}
__device__ __forceinline__ const char* rfl_ptr(const char* p) {
  uint64_t v = (uint64_t)p;
  unsigned lo = __builtin_amdgcn_readfirstlane((unsigned)v), hi = __builtin_amdgcn_readfirstlane((unsigned)(v >> 32));
  return (const char*)(((uint64_t)hi << 32) | lo);
}
__device__ __forceinline__ u32x4 tr_frag(const char* p, int hi_off) {
  s16x4 v0 = __builtin_amdgcn_ds_read_tr16_b64_v4i16((lds_s16x4_p)(p));
  s16x4 v1 = __builtin_amdgcn_ds_read_tr16_b64_v4i16((lds_s16x4_p)(p + hi_off));
  u32x2 a0 = __builtin_bit_cast(u32x2, v0), a1 = __builtin_bit_cast(u32x2, v1);
  return u32x4{a0[0], a0[1], a1[0], a1[1]};
}

__device__ __forceinline__ void dma4(unsigned m0v, __amdgpu_buffer_rsrc_t r, unsigned soff,
                                     unsigned v0, unsigned v1, unsigned v2, unsigned v3) {
  asm volatile(
      "s_mov_b32 m0, %0\n\ts_nop 0\n\t"
      "buffer_load_dwordx4 %1, %5, %6 offen nt lds\n\t"
      "buffer_load_dwordx4 %2, %5, %6 offen offset:1024 nt lds\n\t"
      "buffer_load_dwordx4 %3, %5, %6 offen offset:2048 nt lds\n\t"
      "buffer_load_dwordx4 %4, %5, %6 offen offset:3072 nt lds"
      :: "s"(m0v), "v"(v0), "v"(v1), "v"(v2), "v"(v3), "s"(r), "s"(soff) : "memory");
}
__device__ __forceinline__ void dma2(unsigned m0v, __amdgpu_buffer_rsrc_t r, unsigned soff, unsigned v0, unsigned v1) {
  asm volatile(
      "s_mov_b32 m0, %0\n\ts_nop 0\n\t"
      "buffer_load_dwordx4 %1, %3, %4 offen lds\n\t"
      "buffer_load_dwordx4 %2, %3, %4 offen offset:1024 lds"
      :: "s"(m0v), "v"(v0), "v"(v1), "s"(r), "s"(soff) : "memory");
}
#define WQPOL " nt"
__device__ __forceinline__ void dma2w(unsigned m0v, __amdgpu_buffer_rsrc_t r, unsigned soff, unsigned v0, unsigned v1) {
  asm volatile(
      "s_mov_b32 m0, %0\n\ts_nop 0\n\t"
      "buffer_load_dwordx4 %1, %3, %4 offen" WQPOL " lds\n\t"
      "buffer_load_dwordx4 %2, %3, %4 offen offset:1024" WQPOL " lds"
      :: "s"(m0v), "v"(v0), "v"(v1), "s"(r), "s"(soff) : "memory");
}
template <int N> __device__ __forceinline__ void wait_vm() {
  asm volatile("s_waitcnt vmcnt(%0) lgkmcnt(0)" :: "n"(N) : "memory");
}
template <int V> struct IC { static constexpr int value = V; };
template <class F> __device__ __forceinline__ void static_for16(F&& f) {
  f(IC<0>{}); f(IC<1>{}); f(IC<2>{}); f(IC<3>{}); f(IC<4>{}); f(IC<5>{}); f(IC<6>{}); f(IC<7>{});
  f(IC<8>{}); f(IC<9>{}); f(IC<10>{}); f(IC<11>{}); f(IC<12>{}); f(IC<13>{}); f(IC<14>{}); f(IC<15>{});
}
constexpr int qslot(int kk) { return kk % QNW; }
constexpr int qwait(int kk, int nx) {
  if (kk >= 15) return 63;
  int n = 0, wend[16] = {}, xend[8] = {};
  for (int i = 0; i < QNW; i++) {
    if (i != 1) { n += nx; xend[(i + 1) / 2] = n; }
    n += 6; wend[i] = n;
  }
  n += PFP < PFN ? PFP : PFN;
  for (int k = 0; k < kk; k++) {
    if (k + QNW < 16) { n += 6; wend[k + QNW] = n; }
    if ((k & 1) && k < 11) { n += nx; xend[(k + 5) / 2] = n; }
    if (pf_at(k)) n += 1;
    if (k == KV0_STEP) n += 12;
  }
  const int xe = nx ? xend[(kk + 1) / 2] : 0;
  const int need = wend[kk + 1] > xe ? wend[kk + 1] : xe;
  return n - need;
}
__device__ __forceinline__ void scale_acc(f32x4& x, float alpha) {
  asm volatile("" : "+a"(x));
  x *= alpha;
  asm volatile("" : "+a"(x));
}

__global__ __launch_bounds__(256, 1) void attn_kernel(
    const char* __restrict__ xA, const char* __restrict__ KA, const char* __restrict__ VA, const char* __restrict__ wqA,
    const char* __restrict__ xB, const char* __restrict__ KB, const char* __restrict__ VB, const char* __restrict__ wqB,
    const char* __restrict__ woA, const char* __restrict__ woB, u32x4* __restrict__ Obuf) {
  extern __shared__ __attribute__((aligned(16))) char smem[];
  const int bid = blockIdx.x, xj = bid & 7, ci = bid >> 3;
  const int w = __builtin_amdgcn_readfirstlane(threadIdx.x >> 6), l = threadIdx.x & 63;
  const int r = l & 15, g = l >> 4;
  const unsigned ldsbase = (unsigned)(uintptr_t)(lds_char_p)smem;
  const int wid = bid * 4 + w; (void)wid;
  STAMP(0, 0);

  const int hM = 4 * xj + (ci & 3), bM = 2 * (ci >> 2);
  const int bG = ci >> 1, kvh = xj;
  const int hG = 4 * kvh + 2 * (ci & 1);
  const size_t U = (size_t)SK * DH * 2;

  __amdgpu_buffer_rsrc_t drs[3];
  unsigned dvo[3][4], dso[3], dm0[3];
#pragma unroll
  for (int e = 0; e < 3; e++) {
    const int G = 3 * w + e, st = G >> 2, isV = (G >> 1) & 1, hf = G & 1;
    const char* base = st == 2 ? (isV ? VA : KA) + (size_t)(bG * 8 + kvh) * U
                               : (isV ? VB : KB) + (size_t)((bM + st) * NH + hM) * U;
    drs[e] = mkrsrc(rfl_ptr(base), (unsigned)U);
    dso[e] = hf * 4096;
    dm0[e] = ldsbase + G * 4096;
#pragma unroll
    for (int i = 0; i < 4; i++) {
      const int sw = isV ? ((4 * (i & 1) + g) << 1) : (4 * g + i);
      dvo[e][i] = g * 256 + ((r ^ sw) << 4);
    }
  }
  auto issue_group = [&](int e, int t, int slot) {
    dma4(dm0[e] + slot * STAGE, drs[e], dso[e] + t * 8192, dvo[e][0], dvo[e][1], dvo[e][2], dvo[e][3]);
  };
  const int tb = w < 2 ? bM + w : bG, th = w < 2 ? hM : hG + (w - 2);
  u32x4 qf[3][4];
  unsigned pfd = 0;
  auto touch = [&](int j) {
    const unsigned tv = (l & 31) * 128 + (l >> 5) * (PFDT * 8192);
    asm volatile("buffer_load_dword %0, %1, %2, %3 offen" PFPOL
                 : "+v"(pfd) : "v"(tv), "s"(drs[j % 3]), "s"(dso[j % 3] + (PFT0 + 2 * (j / 3) * PFDT) * 8192) : "memory");
  };
  {
    __amdgpu_buffer_rsrc_t rwM = mkrsrc(rfl_ptr(wqB + (size_t)hM * DH * 2), (DM - 1) * NH * DH * 2 + DH * 2);
    __amdgpu_buffer_rsrc_t rwG = mkrsrc(rfl_ptr(wqA + (size_t)hG * DH * 2), (DM - 1) * NH * DH * 2 + 2 * DH * 2);
    const unsigned wv0 = (8 * w + g) * (NH * DH * 2) + ((r ^ (g | ((w & 1) << 3))) << 4);
    const unsigned wv1 = wv0 + 4 * (NH * DH * 2) - 1024;
    __amdgpu_buffer_rsrc_t rx = mkrsrc(rfl_ptr((w < 2 ? xB : xA) + (size_t)tb * SQ * DM * 2), SQ * DM * 2);
    const unsigned xv0 = (l >> 3) * (DM * 2) + (((l & 7) ^ g) << 4);
    const unsigned xv1 = (l >> 3) * (DM * 2) + (((l & 7) ^ (4 + g)) << 4) + 8 * (DM * 2) - 1024;
    const int rot = 2 * QROT;
    auto ldw = [&](int kk, int i) {
      const unsigned so = ((kk + rot) & 15) * 32 * (NH * DH * 2), m = ldsbase + QW_OFF + qslot(kk) * QW_SLOT + w * 2048;
      dma2w(m + i * 8192, i == 0 ? rwM : rwG, so + (i == 2 ? DH * 2 : 0), wv0, wv1);
    };
    auto ldx = [&](int j, int i) {
      const unsigned so = ((2 * j + rot) & 15) * 64, m = ldsbase + QX_OFF + (j % QNX) * QX_SLOT + w * 6144;
      if (w < 3) dma2(m + i * 2048, rx, so + i * 16 * (DM * 2), xv0, xv1);
    };
#pragma unroll
    for (int k = 0; k < QNW; k++) {
#pragma unroll
      for (int i = 0; i < 3; i++) if (k != 1) ldx((k + 1) / 2, i);
#pragma unroll
      for (int i = 0; i < 3; i++) ldw(k, i);
    }
#pragma unroll
    for (int j = 0; j < PFP && j < PFN; j++) touch(j);
    f32x4 qa[8][3];
#pragma unroll
    for (int i = 0; i < 8; i++)
#pragma unroll
      for (int qb = 0; qb < 3; qb++) qa[i][qb] = f32x4{0.f, 0.f, 0.f, 0.f};
    const unsigned abase = QW_OFF + (w < 2 ? 0 : w - 1) * 8192 + (8 * g + (r >> 2)) * 256 + ((r & 2) << 2);
    const unsigned asw = (r >> 2) | ((g & 1) << 3), ach = 4 * (r & 3);
    const unsigned xbase = QX_OFF + (w < 2 ? w : 2) * 6144 + r * 128 + ((g ^ (r >> 1)) << 4);
    u32x4 af[2][8], xf[2][3];
    auto rdx = [&](int kk, int qb) {
      xf[kk & 1][qb] = *(const u32x4*)(smem + ((kk >> 1) % QNX) * QX_SLOT + ((xbase + qb * 2048) ^ ((kk & 1) << 6)));
    };
    auto rda = [&](int kk, int i) {
      af[kk & 1][i] = tr_frag(smem + qslot(kk) * QW_SLOT + ((abase + (((ach + (i >> 1)) ^ asw) << 4)) ^ ((i & 1) << 3)), 1024);
    };
    if (w < 3) wait_vm<qwait(-1, 6)>(); else wait_vm<qwait(-1, 0)>();
    asm volatile("s_barrier" ::: "memory");
#pragma unroll
    for (int qb = 0; qb < 3; qb++) rdx(0, qb);
#pragma unroll
    for (int i = 0; i < 8; i++) rda(0, i);
    static_for16([&](auto kc) {
      constexpr int kk = decltype(kc)::value;
      if (w < 3) wait_vm<qwait(kk, 6)>(); else wait_vm<qwait(kk, 0)>();
      asm volatile("s_barrier" ::: "memory");
#pragma unroll
      for (int i = 0; i < 8; i++) {
        if (kk < 15) {
          if (i < 3) rdx(kk + 1, i);
          rda(kk + 1, i);
        }
#pragma unroll
        for (int qb = 0; qb < 3; qb++) qa[i][qb] = mfma16(af[kk & 1][i], xf[kk & 1][qb], qa[i][qb]);
        if (kk + QNW < 16 && i < 3) ldw(kk + QNW, i);
        if ((kk & 1) && kk < 11 && i >= 3 && i < 6) ldx((kk + 5) / 2, i - 3);
        if (pf_at(kk) && i == 6) touch(pf_at(kk));
        if (kk == KV0_STEP && i < 3) issue_group(i, 0, 0);
        if (kk == 15 && i >= 5) issue_group(i - 5, 1, 1);
        __builtin_amdgcn_sched_barrier(0);
      }
    });
    const bool odd = g >> 1;
#pragma unroll
    for (int qb = 0; qb < 3; qb++)
#pragma unroll
      for (int s = 0; s < 4; s++) {
        const unsigned a0 = pack2(qa[2 * s][qb][0], qa[2 * s][qb][1]), a1 = pack2(qa[2 * s][qb][2], qa[2 * s][qb][3]);
        const unsigned b0 = pack2(qa[2 * s + 1][qb][0], qa[2 * s + 1][qb][1]), b1 = pack2(qa[2 * s + 1][qb][2], qa[2 * s + 1][qb][3]);
        qf[qb][s] = u32x4{odd ? b0 : a0, odd ? b1 : a1, odd ? a0 : b0, odd ? a1 : b1};
      }
  }
#pragma unroll
  for (int e = 0; e < 3; e++) issue_group(e, 2, 2);
  STAMP(0, 1);

  const int strm = w < 2 ? w : 2;
  unsigned kaddr[4], vaddr0;
#pragma unroll
  for (int s = 0; s < 4; s++) kaddr[s] = strm * 16384 + r * 256 + (((4 * g + s) ^ (4 * (r & 3) + (r >> 2))) << 4);
  {
    const int row = 4 * g + (r >> 2), p = r & 3;
    vaddr0 = strm * 16384 + 8192 + row * 256 + ((((p >> 1)) ^ ((row & 7) << 1)) << 4) + (p & 1) * 8;
  }
  f32x4 oa[8][3];
#pragma unroll
  for (int i = 0; i < 8; i++)
#pragma unroll
    for (int qb = 0; qb < 3; qb++) oa[i][qb] = f32x4{0.f, 0.f, 0.f, 0.f};
  float mrow[3] = {-INFINITY, -INFINITY, -INFINITY}, lsum[3] = {0.f, 0.f, 0.f};

  asm volatile("s_waitcnt vmcnt(24) lgkmcnt(0)\n\ts_barrier" :: "v"(pfd) : "memory");
  int slot = 0;
  for (int t = 0; t < NT; t++) {
    const bool refill = t >= 1 && t + 2 < NT;
    const int fs = slot == 0 ? 2 : slot - 1;
    const char* sb = smem + slot * STAGE;
    u32x4 kf[2][4], vf[8];
#pragma unroll
    for (int s = 0; s < 4; s++)
#pragma unroll
      for (int kb = 0; kb < 2; kb++) kf[kb][s] = *(const u32x4*)(sb + kaddr[s] + kb * 4096);
    if (refill) issue_group(0, t + 2, fs);
    __builtin_amdgcn_sched_barrier(0);
    f32x4 sa[2][3];
#pragma unroll
    for (int s = 0; s < 4; s++) {
#pragma unroll
      for (int kb = 0; kb < 2; kb++)
#pragma unroll
        for (int qb = 0; qb < 3; qb++)
          sa[kb][qb] = mfma16(kf[kb][s], qf[qb][s], s == 0 ? f32x4{0.f, 0.f, 0.f, 0.f} : sa[kb][qb]);
      vf[2 * s] = tr_frag(sb + (vaddr0 ^ ((2 * s) << 5)), 4096);
      vf[2 * s + 1] = tr_frag(sb + (vaddr0 ^ ((2 * s + 1) << 5)), 4096);
      __builtin_amdgcn_sched_barrier(0);
    }
    if (refill) issue_group(1, t + 2, fs);
    if (t == WO_TILE) {
      __amdgpu_buffer_rsrc_t rp = mkrsrc(rfl_ptr((bid & 128 ? woB : woA) + (size_t)((bid & 127) * 4 + w) * 8192), 8192);
      asm volatile("s_mov_b32 m0, %0\n\ts_nop 0\n\tbuffer_load_dword %1, %2, 0 offen lds"
                   :: "s"(ldsbase + NSLOT * STAGE + w * 256), "v"(l * 128), "s"(rp) : "memory");
    }
    float mx[3];
    bool need = false;
#pragma unroll
    for (int qb = 0; qb < 3; qb++) {
      float m0 = __builtin_fmaxf(__builtin_fmaxf(sa[0][qb][0], sa[0][qb][1]), sa[0][qb][2]);
      m0 = __builtin_fmaxf(__builtin_fmaxf(m0, sa[0][qb][3]), sa[1][qb][0]);
      m0 = __builtin_fmaxf(__builtin_fmaxf(m0, sa[1][qb][1]), sa[1][qb][2]);
      mx[qb] = __builtin_fmaxf(m0, sa[1][qb][3]);
      need |= mx[qb] > mrow[qb] + RESCALE_THR;
    }
    if (__builtin_expect(__builtin_amdgcn_ballot_w64(need) != 0, 0)) {
#pragma unroll
      for (int qb = 0; qb < 3; qb++) {
        float m = mx[qb];
        m = fmaxf(m, __shfl_xor(m, 16));
        m = fmaxf(m, __shfl_xor(m, 32));
        m = fmaxf(m, mrow[qb]);
        const float alpha = __builtin_amdgcn_exp2f((mrow[qb] - m) * SCL2);
        mrow[qb] = m;
        lsum[qb] *= alpha;
#pragma unroll
        for (int i = 0; i < 8; i++) scale_acc(oa[i][qb], alpha);
      }
    }
    u32x4 pf[3];
#pragma unroll
    for (int qb = 0; qb < 3; qb++) {
      const float mc = mrow[qb] * SCL2;
      float p[8];
#pragma unroll
      for (int kb = 0; kb < 2; kb++)
#pragma unroll
        for (int e = 0; e < 4; e++) p[kb * 4 + e] = __builtin_amdgcn_exp2f(__builtin_fmaf(sa[kb][qb][e], SCL2, -mc));
      lsum[qb] += ((p[0] + p[1]) + (p[2] + p[3])) + ((p[4] + p[5]) + (p[6] + p[7]));
#pragma unroll
      for (int e = 0; e < 4; e++) pf[qb][e] = pack2(p[2 * e], p[2 * e + 1]);
    }
    if (refill) issue_group(2, t + 2, fs);
#pragma unroll
    for (int qb = 0; qb < 3; qb++)
#pragma unroll
      for (int db = 0; db < 8; db++) oa[db][qb] = mfma16(vf[db], pf[qb], oa[db][qb]);
    slot = slot == 2 ? 0 : slot + 1;
    if (t >= NT - 2) asm volatile("s_waitcnt vmcnt(0) lgkmcnt(0)\n\ts_barrier" ::: "memory");
    else asm volatile("s_waitcnt vmcnt(12) lgkmcnt(0)\n\ts_barrier" ::: "memory");
  }
  STAMP(0, 10);

  const int task = ((w < 2 ? 1 : 0) * NB + tb) * NH + th;
#pragma unroll
  for (int qb = 0; qb < 3; qb++) {
    float s = lsum[qb];
    s += __shfl_xor(s, 16);
    s += __shfl_xor(s, 32);
    const float inv = 1.0f / s;
#pragma unroll
    for (int u = 0; u < 4; u++) {
      u32x4 o;
      o[0] = pack2(oa[2 * u][qb][0] * inv, oa[2 * u][qb][1] * inv);
      o[1] = pack2(oa[2 * u][qb][2] * inv, oa[2 * u][qb][3] * inv);
      o[2] = pack2(oa[2 * u + 1][qb][0] * inv, oa[2 * u + 1][qb][1] * inv);
      o[3] = pack2(oa[2 * u + 1][qb][2] * inv, oa[2 * u + 1][qb][3] * inv);
      Obuf[((task * 4 + u) * 3 + qb) * 64 + l] = o;
    }
  }
  STAMP(0, 11);
}

constexpr int OQD = 8;
constexpr int OP_LDS = 4 * OQD * 4096;
__global__ __launch_bounds__(256, 1) void oproj_kernel(const u32x4* __restrict__ Obuf, const char* __restrict__ woA,
                                                       const char* __restrict__ woB, char* __restrict__ out) {
  extern __shared__ __attribute__((aligned(16))) char smem[];
  const int bid = blockIdx.x, xj = bid & 7, ci = bid >> 3;
  const int br = xj >> 2, b = 8 * ((xj >> 1) & 1) + (ci >> 2), ns = NSMAP;
  const int w = __builtin_amdgcn_readfirstlane(threadIdx.x >> 6), l = threadIdx.x & 63;
  const int r = l & 15, g = l >> 4;
  const int wid = bid * 4 + w; (void)wid;
  STAMP(1, 0);
  const unsigned ring = (unsigned)(uintptr_t)(lds_char_p)smem + w * (OQD * 4096);
  const char* wp = rfl_ptr((br ? woB : woA) + (size_t)(w * 1024) * (DM * 2) + ns * 128);
  __amdgpu_buffer_rsrc_t rw = mkrsrc(wp, 1023 * DM * 2 + 128);
  const char* op = rfl_ptr((const char*)(Obuf + (size_t)((br * NB + b) * NH + w * 8) * 12 * 64));
  __amdgpu_buffer_rsrc_t ro = mkrsrc(op, 8 * 12 * 1024);
  unsigned wvo[4];
#pragma unroll
  for (int i = 0; i < 4; i++) wvo[i] = (8 * i + (l >> 3)) * (DM * 2) - 1024 * i + (((l & 7) ^ (((l >> 4) & 3) << 1)) << 4);
  const unsigned ovo = l * 16;
  u32x4 ob[OQD][3];
  f32x4 acc[4][3];
#pragma unroll
  for (int i = 0; i < 4; i++)
#pragma unroll
    for (int qb = 0; qb < 3; qb++) acc[i][qb] = f32x4{0.f, 0.f, 0.f, 0.f};
  const int rot = OROT;
  auto ldw = [&](int kk, int sl) {
    asm volatile(
        "s_mov_b32 m0, %0\n\ts_nop 0\n\t"
        "buffer_load_dwordx4 %1, %5, %6 offen lds\n\t"
        "buffer_load_dwordx4 %2, %5, %6 offen offset:1024 lds\n\t"
        "buffer_load_dwordx4 %3, %5, %6 offen offset:2048 lds\n\t"
        "buffer_load_dwordx4 %4, %5, %6 offen offset:3072 lds"
        :: "s"(ring + sl * 4096), "v"(wvo[0]), "v"(wvo[1]), "v"(wvo[2]), "v"(wvo[3]), "s"(rw),
           "s"(((kk + rot) & 31) * 32 * DM * 2)
        : "memory");
  };
  auto ldo = [&](int kk, int sl) {
    asm volatile(
        "buffer_load_dwordx4 %0, %3, %4, %5 offen\n\t"
        "buffer_load_dwordx4 %1, %3, %4, %5 offen offset:1024\n\t"
        "buffer_load_dwordx4 %2, %3, %4, %5 offen offset:2048"
        : "=&v"(ob[sl][0]), "=&v"(ob[sl][1]), "=&v"(ob[sl][2])
        : "v"(ovo), "s"(ro), "s"(((kk + rot) & 31) * 3072)
        : "memory");
  };
  const int row = 4 * g + (r >> 2), p = r & 3;
  const unsigned aoff = w * (OQD * 4096) + row * 128 + (((p >> 1) ^ (((row >> 1) & 3) << 1)) << 4) + (p & 1) * 8;
#pragma unroll
  for (int kk = 0; kk < OQD; kk++) { ldw(kk, kk); ldo(kk, kk); }
  u32x4 af[2][4];
  asm volatile("s_waitcnt vmcnt(%0)" :: "n"(7 * OQD - 4) : "memory");
#pragma unroll
  for (int db = 0; db < 4; db++) af[0][db] = tr_frag(smem + (aoff ^ (db << 5)), 2048);
  auto step = [&](auto kc) {
    constexpr int kk = decltype(kc)::value, sl = kk % OQD;
    constexpr int last = kk + OQD - 1 < 31 ? kk + OQD - 1 : 31;
    constexpr int allow = kk < 31 ? 7 * (last - kk - 1) + 3 : 0;
    asm volatile("s_waitcnt vmcnt(%3) lgkmcnt(0)" : "+v"(ob[sl][0]), "+v"(ob[sl][1]), "+v"(ob[sl][2]) : "n"(allow) : "memory");
    if (kk < 31) {
#pragma unroll
      for (int db = 0; db < 4; db++) af[(kk + 1) & 1][db] = tr_frag(smem + ((kk + 1) % OQD) * 4096 + (aoff ^ (db << 5)), 2048);
    }
#pragma unroll
    for (int db = 0; db < 2; db++)
#pragma unroll
      for (int qb = 0; qb < 3; qb++) acc[db][qb] = mfma16(af[kk & 1][db], ob[sl][qb], acc[db][qb]);
    __builtin_amdgcn_sched_barrier(0);
    if (kk + OQD < 32) ldw(kk + OQD, sl);
#pragma unroll
    for (int db = 2; db < 4; db++)
#pragma unroll
      for (int qb = 0; qb < 3; qb++) acc[db][qb] = mfma16(af[kk & 1][db], ob[sl][qb], acc[db][qb]);
    __builtin_amdgcn_sched_barrier(0);
    if (kk + OQD < 32) ldo(kk + OQD, sl);
  };
  static_for16(step);
  STAMP(1, 3);
  static_for16([&](auto kc) { step(IC<16 + decltype(kc)::value>{}); });
  STAMP(1, 1);
  f32x4* red = (f32x4*)smem;
#pragma unroll
  for (int qb = 0; qb < 3; qb++)
#pragma unroll
    for (int db = 0; db < 4; db++) red[w * (OQD * 256) + (qb * 4 + db) * 64 + l] = acc[db][qb];
  __syncthreads();
#pragma unroll
  for (int it = 0; it < 2; it++) {
    const int wv = w + 4 * it;
    if (wv < 6) {
      const int q = 8 * wv + (l >> 3), c8 = l & 7;
      const int qb = q >> 4, idx = (qb * 4 + (c8 >> 1)) * 64 + (2 * (c8 & 1)) * 16 + (q & 15);
      f32x4 s0 = {0.f, 0.f, 0.f, 0.f}, s1 = s0;
#pragma unroll
      for (int ww = 0; ww < 4; ww++) {
        s0 += red[ww * (OQD * 256) + idx];
        s1 += red[ww * (OQD * 256) + idx + 16];
      }
      u32x4 o = {pack2(s0[0], s0[1]), pack2(s0[2], s0[3]), pack2(s1[0], s1[1]), pack2(s1[2], s1[3])};
      *(u32x4*)(out + (size_t)(b * SQ + q) * 2048 + br * 1024 + ns * 128 + c8 * 16) = o;
    }
  }
  STAMP(1, 2);
}

extern "C" void kernel_launch(void* const* d_in, const int* in_sizes, int n_in, void* d_out, int out_size,
                              void* d_ws, size_t ws_size, hipStream_t stream) {
  const char* xA = (const char*)d_in[0]; const char* KA = (const char*)d_in[1]; const char* VA = (const char*)d_in[2];
  const char* wqA = (const char*)d_in[3]; const char* woA = (const char*)d_in[4];
  const char* xB = (const char*)d_in[5]; const char* KB = (const char*)d_in[6]; const char* VB = (const char*)d_in[7];
  const char* wqB = (const char*)d_in[8]; const char* woB = (const char*)d_in[9];
  u32x4* Obuf = (u32x4*)d_ws;
  (void)hipFuncSetAttribute((const void*)attn_kernel, hipFuncAttributeMaxDynamicSharedMemorySize, ATTN_LDS);
  (void)hipFuncSetAttribute((const void*)oproj_kernel, hipFuncAttributeMaxDynamicSharedMemorySize, OP_LDS);
  attn_kernel<<<256, 256, ATTN_LDS, stream>>>(xA, KA, VA, wqA, xB, KB, VB, wqB, woA, woB, Obuf);
  oproj_kernel<<<256, 256, OP_LDS, stream>>>(Obuf, woA, woB, (char*)d_out);
}
```

```cpp
#include <hip/hip_runtime.h>
#include <hip/hip_bf16.h>
#include <stdint.h>

typedef short s16x4 __attribute__((ext_vector_type(4)));
typedef float f32x4 __attribute__((ext_vector_type(4)));
typedef unsigned u32x4 __attribute__((ext_vector_type(4)));
typedef unsigned u32x2 __attribute__((ext_vector_type(2)));
typedef __bf16 bf16x8 __attribute__((ext_vector_type(8)));
typedef __bf16 bf16x2 __attribute__((ext_vector_type(2)));
typedef float f32x2 __attribute__((ext_vector_type(2)));
typedef __attribute__((address_space(3))) s16x4* lds_s16x4_p;
typedef __attribute__((address_space(3))) char* lds_char_p;

constexpr int NB = 16, SQ = 48, DM = 512, NH = 32, SK = 1024, DH = 128;
constexpr int STAGE = 49152;
constexpr int NSLOT = 3;
constexpr int NT = SK / 32;
constexpr int ATTN_LDS = NSLOT * STAGE + 16384;
constexpr float SCL2 = 0.08838834764831845f * 1.4426950408889634f;
constexpr float RESCALE_THR = 8.0f / SCL2;
constexpr int QW_SLOT = 24576, QX_SLOT = 18432;
#define KV0_STEP 13
constexpr int QNW = 4, QNX = 3;
constexpr int WPK_OFF = 1 << 20;
#define PFN 0
#define PFP 0
#define PFS0 2
#define PFDS 3
#define PFT0 5
#define PFDT 7
#define PFPOL " nt"
constexpr int pf_at(int kk) { int n = 0; for (int j = PFP; j < PFN; j++) if (PFS0 + (j - PFP) * PFDS == kk) n = j; return n; }
constexpr int QW_OFF = ATTN_LDS - QNW * QW_SLOT - QNX * QX_SLOT, QX_OFF = ATTN_LDS - QNX * QX_SLOT;
#define MAPV 1
#define NSMAP (4 * (xj & 1) + (ci & 3))
#define WUMAP ((ci * 4 + w) * 2 + ((xj >> 1) & 1))
#define ST_AUX 16
#define SW_AUX 16
#define OUT_AUX 16
#define OROT 0
#define WO_TILE 31
#define QROT (ci >> 2)

#define STAMP(k, i)

__device__ __forceinline__ unsigned pack2(float a, float b) {
  f32x2 v = {a, b};
  bf16x2 r = __builtin_convertvector(v, bf16x2);
  return __builtin_bit_cast(unsigned, r);
}
__device__ __forceinline__ f32x4 mfma16(u32x4 a, u32x4 b, f32x4 c) {
  return __builtin_amdgcn_mfma_f32_16x16x32_bf16(__builtin_bit_cast(bf16x8, a), __builtin_bit_cast(bf16x8, b), c, 0, 0, 0);
}
__device__ __forceinline__ __amdgpu_buffer_rsrc_t mkrsrc(const void* p, unsigned bytes) {
  return __builtin_amdgcn_make_buffer_rsrc((void*)p, 0, bytes, 0x00020000);
}
__device__ __forceinline__ const char* rfl_ptr(const char* p) {
  uint64_t v = (uint64_t)p;
  unsigned lo = __builtin_amdgcn_readfirstlane((unsigned)v), hi = __builtin_amdgcn_readfirstlane((unsigned)(v >> 32));
  return (const char*)(((uint64_t)hi << 32) | lo);
}
__device__ __forceinline__ u32x4 tr_frag(const char* p, int hi_off) {
  s16x4 v0 = __builtin_amdgcn_ds_read_tr16_b64_v4i16((lds_s16x4_p)(p));
  s16x4 v1 = __builtin_amdgcn_ds_read_tr16_b64_v4i16((lds_s16x4_p)(p + hi_off));
  u32x2 a0 = __builtin_bit_cast(u32x2, v0), a1 = __builtin_bit_cast(u32x2, v1);
  return u32x4{a0[0], a0[1], a1[0], a1[1]};
}

__device__ __forceinline__ void dma4(unsigned m0v, __amdgpu_buffer_rsrc_t r, unsigned soff,
                                     unsigned v0, unsigned v1, unsigned v2, unsigned v3) {
  asm volatile(
      "s_mov_b32 m0, %0\n\ts_nop 0\n\t"
      "buffer_load_dwordx4 %1, %5, %6 offen nt lds\n\t"
      "buffer_load_dwordx4 %2, %5, %6 offen offset:1024 nt lds\n\t"
      "buffer_load_dwordx4 %3, %5, %6 offen offset:2048 nt lds\n\t"
      "buffer_load_dwordx4 %4, %5, %6 offen offset:3072 nt lds"
      :: "s"(m0v), "v"(v0), "v"(v1), "v"(v2), "v"(v3), "s"(r), "s"(soff) : "memory");
}
__device__ __forceinline__ void dma2(unsigned m0v, __amdgpu_buffer_rsrc_t r, unsigned soff, unsigned v0, unsigned v1) {
  asm volatile(
      "s_mov_b32 m0, %0\n\ts_nop 0\n\t"
      "buffer_load_dwordx4 %1, %3, %4 offen lds\n\t"
      "buffer_load_dwordx4 %2, %3, %4 offen offset:1024 lds"
      :: "s"(m0v), "v"(v0), "v"(v1), "s"(r), "s"(soff) : "memory");
}
#define WQPOL ""
__device__ __forceinline__ void dma2w(unsigned m0v, __amdgpu_buffer_rsrc_t r, unsigned soff, unsigned v0, unsigned v1) {
  asm volatile(
      "s_mov_b32 m0, %0\n\ts_nop 0\n\t"
      "buffer_load_dwordx4 %1, %3, %4 offen" WQPOL " lds\n\t"
      "buffer_load_dwordx4 %2, %3, %4 offen offset:1024" WQPOL " lds"
      :: "s"(m0v), "v"(v0), "v"(v1), "s"(r), "s"(soff) : "memory");
}
template <int N> __device__ __forceinline__ void wait_vm() {
  asm volatile("s_waitcnt vmcnt(%0) lgkmcnt(0)" :: "n"(N) : "memory");
}
template <int V> struct IC { static constexpr int value = V; };
template <class F> __device__ __forceinline__ void static_for16(F&& f) {
  f(IC<0>{}); f(IC<1>{}); f(IC<2>{}); f(IC<3>{}); f(IC<4>{}); f(IC<5>{}); f(IC<6>{}); f(IC<7>{});
  f(IC<8>{}); f(IC<9>{}); f(IC<10>{}); f(IC<11>{}); f(IC<12>{}); f(IC<13>{}); f(IC<14>{}); f(IC<15>{});
}
constexpr int qslot(int kk) { return kk % QNW; }
constexpr int qwait(int kk, int nx) {
  if (kk >= 15) return 63;
  int n = 0, wend[16] = {}, xend[8] = {};
  for (int i = 0; i < QNW; i++) {
    if (i != 1) { n += nx; xend[(i + 1) / 2] = n; }
    n += 6; wend[i] = n;
  }
  n += PFP < PFN ? PFP : PFN;
  for (int k = 0; k < kk; k++) {
    if (k + QNW < 16) { n += 6; wend[k + QNW] = n; }
    if ((k & 1) && k < 11) { n += nx; xend[(k + 5) / 2] = n; }
    if (pf_at(k)) n += 1;
    if (k == KV0_STEP) n += 12;
  }
  const int xe = nx ? xend[(kk + 1) / 2] : 0;
  const int need = wend[kk + 1] > xe ? wend[kk + 1] : xe;
  return n - need;
}
__device__ __forceinline__ void scale_acc(f32x4& x, float alpha) {
  asm volatile("" : "+a"(x));
  x *= alpha;
  asm volatile("" : "+a"(x));
}

__global__ __launch_bounds__(256, 1) void attn_kernel(
    const char* __restrict__ xA, const char* __restrict__ KA, const char* __restrict__ VA, const char* __restrict__ wqA,
    const char* __restrict__ xB, const char* __restrict__ KB, const char* __restrict__ VB, const char* __restrict__ wqB,
    const char* __restrict__ woA, const char* __restrict__ woB, u32x4* __restrict__ Obuf) {
  extern __shared__ __attribute__((aligned(16))) char smem[];
  const int bid = blockIdx.x, xj = bid & 7, ci = bid >> 3;
  const int w = __builtin_amdgcn_readfirstlane(threadIdx.x >> 6), l = threadIdx.x & 63;
  const int r = l & 15, g = l >> 4;
  const unsigned ldsbase = (unsigned)(uintptr_t)(lds_char_p)smem;
  const int wid = bid * 4 + w; (void)wid;
  STAMP(0, 0);

  const int hM = 4 * xj + (ci & 3), bM = 2 * (ci >> 2);
  const int bG = ci >> 1, kvh = xj;
  const int hG = 4 * kvh + 2 * (ci & 1);
  const size_t U = (size_t)SK * DH * 2;

  __amdgpu_buffer_rsrc_t drs[3];
  unsigned dvo[3][4], dso[3], dm0[3];
#pragma unroll
  for (int e = 0; e < 3; e++) {
    const int G = 3 * w + e, st = G >> 2, isV = (G >> 1) & 1, hf = G & 1;
    const char* base = st == 2 ? (isV ? VA : KA) + (size_t)(bG * 8 + kvh) * U
                               : (isV ? VB : KB) + (size_t)((bM + st) * NH + hM) * U;
    drs[e] = mkrsrc(rfl_ptr(base), (unsigned)U);
    dso[e] = hf * 4096;
    dm0[e] = ldsbase + G * 4096;
#pragma unroll
    for (int i = 0; i < 4; i++) {
      const int sw = isV ? ((4 * (i & 1) + g) << 1) : (4 * g + i);
      dvo[e][i] = g * 256 + ((r ^ sw) << 4);
    }
  }
  auto issue_group = [&](int e, int t, int slot) {
    dma4(dm0[e] + slot * STAGE, drs[e], dso[e] + t * 8192, dvo[e][0], dvo[e][1], dvo[e][2], dvo[e][3]);
  };
  const int tb = w < 2 ? bM + w : bG, th = w < 2 ? hM : hG + (w - 2);
  u32x4 qf[3][4];
  unsigned pfd = 0;
  auto touch = [&](int j) {
    const unsigned tv = (l & 31) * 128 + (l >> 5) * (PFDT * 8192);
    asm volatile("buffer_load_dword %0, %1, %2, %3 offen" PFPOL
                 : "+v"(pfd) : "v"(tv), "s"(drs[j % 3]), "s"(dso[j % 3] + (PFT0 + 2 * (j / 3) * PFDT) * 8192) : "memory");
  };
  {
    __amdgpu_buffer_rsrc_t rwM = mkrsrc(rfl_ptr(wqB + (size_t)hM * DH * 2), (DM - 1) * NH * DH * 2 + DH * 2);
    __amdgpu_buffer_rsrc_t rwG = mkrsrc(rfl_ptr(wqA + (size_t)hG * DH * 2), (DM - 1) * NH * DH * 2 + 2 * DH * 2);
    const unsigned wv0 = (8 * w + g) * (NH * DH * 2) + ((r ^ (g | ((w & 1) << 3))) << 4);
    const unsigned wv1 = wv0 + 4 * (NH * DH * 2) - 1024;
    __amdgpu_buffer_rsrc_t rx = mkrsrc(rfl_ptr((w < 2 ? xB : xA) + (size_t)tb * SQ * DM * 2), SQ * DM * 2);
    const unsigned xv0 = (l >> 3) * (DM * 2) + (((l & 7) ^ g) << 4);
    const unsigned xv1 = (l >> 3) * (DM * 2) + (((l & 7) ^ (4 + g)) << 4) + 8 * (DM * 2) - 1024;
    const int rot = 2 * QROT;
    auto ldw = [&](int kk, int i) {
      const unsigned so = ((kk + rot) & 15) * 32 * (NH * DH * 2), m = ldsbase + QW_OFF + qslot(kk) * QW_SLOT + w * 2048;
      dma2w(m + i * 8192, i == 0 ? rwM : rwG, so + (i == 2 ? DH * 2 : 0), wv0, wv1);
    };
    auto ldx = [&](int j, int i) {
      const unsigned so = ((2 * j + rot) & 15) * 64, m = ldsbase + QX_OFF + (j % QNX) * QX_SLOT + w * 6144;
      if (w < 3) dma2(m + i * 2048, rx, so + i * 16 * (DM * 2), xv0, xv1);
    };
#pragma unroll
    for (int k = 0; k < QNW; k++) {
#pragma unroll
      for (int i = 0; i < 3; i++) if (k != 1) ldx((k + 1) / 2, i);
#pragma unroll
      for (int i = 0; i < 3; i++) ldw(k, i);
    }
#pragma unroll
    for (int j = 0; j < PFP && j < PFN; j++) touch(j);
    f32x4 qa[8][3];
#pragma unroll
    for (int i = 0; i < 8; i++)
#pragma unroll
      for (int qb = 0; qb < 3; qb++) qa[i][qb] = f32x4{0.f, 0.f, 0.f, 0.f};
    const unsigned abase = QW_OFF + (w < 2 ? 0 : w - 1) * 8192 + (8 * g + (r >> 2)) * 256 + ((r & 2) << 2);
    const unsigned asw = (r >> 2) | ((g & 1) << 3), ach = 4 * (r & 3);
    const unsigned xbase = QX_OFF + (w < 2 ? w : 2) * 6144 + r * 128 + ((g ^ (r >> 1)) << 4);
    u32x4 af[2][8], xf[2][3];
    auto rdx = [&](int kk, int qb) {
      xf[kk & 1][qb] = *(const u32x4*)(smem + ((kk >> 1) % QNX) * QX_SLOT + ((xbase + qb * 2048) ^ ((kk & 1) << 6)));
    };
    auto rda = [&](int kk, int i) {
      af[kk & 1][i] = tr_frag(smem + qslot(kk) * QW_SLOT + ((abase + (((ach + (i >> 1)) ^ asw) << 4)) ^ ((i & 1) << 3)), 1024);
    };
    if (w < 3) wait_vm<qwait(-1, 6)>(); else wait_vm<qwait(-1, 0)>();
    asm volatile("s_barrier" ::: "memory");
#pragma unroll
    for (int qb = 0; qb < 3; qb++) rdx(0, qb);
#pragma unroll
    for (int i = 0; i < 8; i++) rda(0, i);
    static_for16([&](auto kc) {
      constexpr int kk = decltype(kc)::value;
      if (w < 3) wait_vm<qwait(kk, 6)>(); else wait_vm<qwait(kk, 0)>();
      asm volatile("s_barrier" ::: "memory");
#pragma unroll
      for (int i = 0; i < 8; i++) {
        if (kk < 15) {
          if (i < 3) rdx(kk + 1, i);
          rda(kk + 1, i);
        }
#pragma unroll
        for (int qb = 0; qb < 3; qb++) qa[i][qb] = mfma16(af[kk & 1][i], xf[kk & 1][qb], qa[i][qb]);
        if (kk + QNW < 16 && i < 3) ldw(kk + QNW, i);
        if ((kk & 1) && kk < 11 && i >= 3 && i < 6) ldx((kk + 5) / 2, i - 3);
        if (pf_at(kk) && i == 6) touch(pf_at(kk));
        if (kk == KV0_STEP && i < 3) issue_group(i, 0, 0);
        if (kk == 15 && i >= 5) issue_group(i - 5, 1, 1);
        __builtin_amdgcn_sched_barrier(0);
      }
    });
    const bool odd = g >> 1;
#pragma unroll
    for (int qb = 0; qb < 3; qb++)
#pragma unroll
      for (int s = 0; s < 4; s++) {
        const unsigned a0 = pack2(qa[2 * s][qb][0], qa[2 * s][qb][1]), a1 = pack2(qa[2 * s][qb][2], qa[2 * s][qb][3]);
        const unsigned b0 = pack2(qa[2 * s + 1][qb][0], qa[2 * s + 1][qb][1]), b1 = pack2(qa[2 * s + 1][qb][2], qa[2 * s + 1][qb][3]);
        qf[qb][s] = u32x4{odd ? b0 : a0, odd ? b1 : a1, odd ? a0 : b0, odd ? a1 : b1};
      }
  }
#pragma unroll
  for (int e = 0; e < 3; e++) issue_group(e, 2, 2);
  STAMP(0, 1);

  const int strm = w < 2 ? w : 2;
  unsigned kaddr[4], vaddr0;
#pragma unroll
  for (int s = 0; s < 4; s++) kaddr[s] = strm * 16384 + r * 256 + (((4 * g + s) ^ (4 * (r & 3) + (r >> 2))) << 4);
  {
    const int row = 4 * g + (r >> 2), p = r & 3;
    vaddr0 = strm * 16384 + 8192 + row * 256 + ((((p >> 1)) ^ ((row & 7) << 1)) << 4) + (p & 1) * 8;
  }
  f32x4 oa[8][3];
#pragma unroll
  for (int i = 0; i < 8; i++)
#pragma unroll
    for (int qb = 0; qb < 3; qb++) oa[i][qb] = f32x4{0.f, 0.f, 0.f, 0.f};
  float mrow[3] = {-INFINITY, -INFINITY, -INFINITY}, lsum[3] = {0.f, 0.f, 0.f};

  asm volatile("s_waitcnt vmcnt(24) lgkmcnt(0)\n\ts_barrier" :: "v"(pfd) : "memory");
  int slot = 0;
  const int wu = WUMAP;
  for (int t = 0; t < NT; t++) {
    const bool refill = t + 3 < NT;
    const char* sb = smem + slot * STAGE;
    u32x4 kf[2][4], vf[8];
#pragma unroll
    for (int s = 0; s < 4; s++)
#pragma unroll
      for (int kb = 0; kb < 2; kb++) kf[kb][s] = *(const u32x4*)(sb + kaddr[s] + kb * 4096);
    __builtin_amdgcn_sched_barrier(0);
    f32x4 sa[2][3];
#pragma unroll
    for (int s = 0; s < 4; s++) {
#pragma unroll
      for (int kb = 0; kb < 2; kb++)
#pragma unroll
        for (int qb = 0; qb < 3; qb++)
          sa[kb][qb] = mfma16(kf[kb][s], qf[qb][s], s == 0 ? f32x4{0.f, 0.f, 0.f, 0.f} : sa[kb][qb]);
      vf[2 * s] = tr_frag(sb + (vaddr0 ^ ((2 * s) << 5)), 4096);
      vf[2 * s + 1] = tr_frag(sb + (vaddr0 ^ ((2 * s + 1) << 5)), 4096);
      __builtin_amdgcn_sched_barrier(0);
    }
    asm volatile("s_waitcnt lgkmcnt(0)\n\ts_barrier" ::: "memory");
    if (refill) { issue_group(0, t + 3, slot); issue_group(1, t + 3, slot); }
    if (t == NT - 3) {
      __amdgpu_buffer_rsrc_t rp = mkrsrc(rfl_ptr((xj & 4 ? woB : woA) + (size_t)wu * 16384 + (xj & 1) * 512 - 3072), 18944);
      const unsigned pv = 3072 + (l >> 3) * 1024 + (l & 7) * 16;
      dma4(ldsbase + 2 * STAGE + w * 8192, rp, 0, pv, pv - 896, pv - 1792, pv - 2688);
      dma4(ldsbase + 2 * STAGE + w * 8192 + 4096, rp, 8192, pv, pv - 896, pv - 1792, pv - 2688);
    }
    float mx[3];
    bool need = false;
#pragma unroll
    for (int qb = 0; qb < 3; qb++) {
      float m0 = __builtin_fmaxf(__builtin_fmaxf(sa[0][qb][0], sa[0][qb][1]), sa[0][qb][2]);
      m0 = __builtin_fmaxf(__builtin_fmaxf(m0, sa[0][qb][3]), sa[1][qb][0]);
      m0 = __builtin_fmaxf(__builtin_fmaxf(m0, sa[1][qb][1]), sa[1][qb][2]);
      mx[qb] = __builtin_fmaxf(m0, sa[1][qb][3]);
      need |= mx[qb] > mrow[qb] + RESCALE_THR;
    }
    if (__builtin_expect(__builtin_amdgcn_ballot_w64(need) != 0, 0)) {
#pragma unroll
      for (int qb = 0; qb < 3; qb++) {
        float m = mx[qb];
        m = fmaxf(m, __shfl_xor(m, 16));
        m = fmaxf(m, __shfl_xor(m, 32));
        m = fmaxf(m, mrow[qb]);
        const float alpha = __builtin_amdgcn_exp2f((mrow[qb] - m) * SCL2);
        mrow[qb] = m;
        lsum[qb] *= alpha;
#pragma unroll
        for (int i = 0; i < 8; i++) scale_acc(oa[i][qb], alpha);
      }
    }
    u32x4 pf[3];
#pragma unroll
    for (int qb = 0; qb < 3; qb++) {
      const float mc = mrow[qb] * SCL2;
      float p[8];
#pragma unroll
      for (int kb = 0; kb < 2; kb++)
#pragma unroll
        for (int e = 0; e < 4; e++) p[kb * 4 + e] = __builtin_amdgcn_exp2f(__builtin_fmaf(sa[kb][qb][e], SCL2, -mc));
      lsum[qb] += ((p[0] + p[1]) + (p[2] + p[3])) + ((p[4] + p[5]) + (p[6] + p[7]));
#pragma unroll
      for (int e = 0; e < 4; e++) pf[qb][e] = pack2(p[2 * e], p[2 * e + 1]);
    }
    if (refill) issue_group(2, t + 3, slot);
#pragma unroll
    for (int qb = 0; qb < 3; qb++)
#pragma unroll
      for (int db = 0; db < 8; db++) oa[db][qb] = mfma16(vf[db], pf[qb], oa[db][qb]);
    slot = slot == 2 ? 0 : slot + 1;
    if (t >= NT - 2) asm volatile("s_waitcnt vmcnt(0) lgkmcnt(0)\n\ts_barrier" ::: "memory");
    else if (t == NT - 3) asm volatile("s_waitcnt vmcnt(20) lgkmcnt(0)\n\ts_barrier" ::: "memory");
    else asm volatile("s_waitcnt vmcnt(24) lgkmcnt(0)\n\ts_barrier" ::: "memory");
  }
  STAMP(0, 10);

  __amdgpu_buffer_rsrc_t rsw = mkrsrc(rfl_ptr((const char*)(Obuf + WPK_OFF + ((xj >> 2) * 8 + (xj & 1) * 4) * 32768 + wu * 128)),
                                      3 * 524288 + 2048);
#pragma unroll
  for (int i = 0; i < 8; i++)
    __builtin_amdgcn_raw_buffer_store_b128(*(const u32x4*)(smem + 2 * STAGE + w * 8192 + i * 1024 + l * 16), rsw, l * 16,
                                           (i & 3) * 524288 + (i >> 2) * 1024, SW_AUX);
  const int task = ((w < 2 ? 1 : 0) * NB + tb) * NH + th;
  __amdgpu_buffer_rsrc_t rso = mkrsrc(rfl_ptr((const char*)(Obuf + (size_t)task * 12 * 64)), 12 * 1024);
#pragma unroll
  for (int qb = 0; qb < 3; qb++) {
    float s = lsum[qb];
    s += __shfl_xor(s, 16);
    s += __shfl_xor(s, 32);
    const float inv = 1.0f / s;
#pragma unroll
    for (int u = 0; u < 4; u++) {
      u32x4 o;
      o[0] = pack2(oa[2 * u][qb][0] * inv, oa[2 * u][qb][1] * inv);
      o[1] = pack2(oa[2 * u][qb][2] * inv, oa[2 * u][qb][3] * inv);
      o[2] = pack2(oa[2 * u + 1][qb][0] * inv, oa[2 * u + 1][qb][1] * inv);
      o[3] = pack2(oa[2 * u + 1][qb][2] * inv, oa[2 * u + 1][qb][3] * inv);
      __builtin_amdgcn_raw_buffer_store_b128(o, rso, l * 16, (u * 3 + qb) * 1024, ST_AUX);
    }
  }
  STAMP(0, 11);
}

constexpr int OQD = 8;
constexpr int OP_LDS = 4 * OQD * 4096;
__global__ __launch_bounds__(256, 1) void oproj_kernel(const u32x4* __restrict__ Obuf, char* __restrict__ out) {
  extern __shared__ __attribute__((aligned(16))) char smem[];
  const int bid = blockIdx.x, xj = bid & 7, ci = bid >> 3;
  const int br = xj >> 2, b = 8 * ((xj >> 1) & 1) + (ci >> 2), ns = NSMAP;
  const int w = __builtin_amdgcn_readfirstlane(threadIdx.x >> 6), l = threadIdx.x & 63;
  const int r = l & 15, g = l >> 4;
  const int wid = bid * 4 + w; (void)wid;
  STAMP(1, 0);
  const unsigned ring = (unsigned)(uintptr_t)(lds_char_p)smem + w * (OQD * 4096);
  const char* wp = rfl_ptr((const char*)(Obuf + WPK_OFF + (br * 8 + ns) * 32768 + w * 8192));
  __amdgpu_buffer_rsrc_t rw = mkrsrc(wp, 1024 * 128);
  const char* op = rfl_ptr((const char*)(Obuf + (size_t)((br * NB + b) * NH + w * 8) * 12 * 64));
  __amdgpu_buffer_rsrc_t ro = mkrsrc(op, 8 * 12 * 1024);
  const unsigned wvo = (l >> 3) * 128 + (((l & 7) ^ (((l >> 4) & 3) << 1)) << 4);
  const unsigned ovo = l * 16;
  u32x4 ob[OQD][3];
  f32x4 acc[4][3];
#pragma unroll
  for (int i = 0; i < 4; i++)
#pragma unroll
    for (int qb = 0; qb < 3; qb++) acc[i][qb] = f32x4{0.f, 0.f, 0.f, 0.f};
  const int rot = OROT;
  auto ldw = [&](int kk, int sl) {
    asm volatile(
        "s_mov_b32 m0, %0\n\ts_nop 0\n\t"
        "buffer_load_dwordx4 %1, %5, %6 offen lds\n\t"
        "buffer_load_dwordx4 %2, %5, %6 offen offset:1024 lds\n\t"
        "buffer_load_dwordx4 %3, %5, %6 offen offset:2048 lds\n\t"
        "buffer_load_dwordx4 %4, %5, %6 offen offset:3072 lds"
        :: "s"(ring + sl * 4096), "v"(wvo), "v"(wvo), "v"(wvo), "v"(wvo), "s"(rw),
           "s"(((kk + rot) & 31) * 4096)
        : "memory");
  };
  auto ldo = [&](int kk, int sl) {
    asm volatile(
        "buffer_load_dwordx4 %0, %3, %4, %5 offen\n\t"
        "buffer_load_dwordx4 %1, %3, %4, %5 offen offset:1024\n\t"
        "buffer_load_dwordx4 %2, %3, %4, %5 offen offset:2048"
        : "=&v"(ob[sl][0]), "=&v"(ob[sl][1]), "=&v"(ob[sl][2])
        : "v"(ovo), "s"(ro), "s"(((kk + rot) & 31) * 3072)
        : "memory");
  };
  const int row = 4 * g + (r >> 2), p = r & 3;
  const unsigned aoff = w * (OQD * 4096) + row * 128 + (((p >> 1) ^ (((row >> 1) & 3) << 1)) << 4) + (p & 1) * 8;
#pragma unroll
  for (int kk = 0; kk < OQD; kk++) { ldw(kk, kk); ldo(kk, kk); }
  u32x4 af[2][4];
  asm volatile("s_waitcnt vmcnt(%0)" :: "n"(7 * OQD - 4) : "memory");
#pragma unroll
  for (int db = 0; db < 4; db++) af[0][db] = tr_frag(smem + (aoff ^ (db << 5)), 2048);
  auto step = [&](auto kc) {
    constexpr int kk = decltype(kc)::value, sl = kk % OQD;
    constexpr int last = kk + OQD - 1 < 31 ? kk + OQD - 1 : 31;
    constexpr int allow = kk < 31 ? 7 * (last - kk - 1) + 3 : 0;
    asm volatile("s_waitcnt vmcnt(%3) lgkmcnt(0)" : "+v"(ob[sl][0]), "+v"(ob[sl][1]), "+v"(ob[sl][2]) : "n"(allow) : "memory");
    if (kk < 31) {
#pragma unroll
      for (int db = 0; db < 4; db++) af[(kk + 1) & 1][db] = tr_frag(smem + ((kk + 1) % OQD) * 4096 + (aoff ^ (db << 5)), 2048);
    }
#pragma unroll
    for (int db = 0; db < 2; db++)
#pragma unroll
      for (int qb = 0; qb < 3; qb++) acc[db][qb] = mfma16(af[kk & 1][db], ob[sl][qb], acc[db][qb]);
    __builtin_amdgcn_sched_barrier(0);
    if (kk + OQD < 32) ldw(kk + OQD, sl);
#pragma unroll
    for (int db = 2; db < 4; db++)
#pragma unroll
      for (int qb = 0; qb < 3; qb++) acc[db][qb] = mfma16(af[kk & 1][db], ob[sl][qb], acc[db][qb]);
    __builtin_amdgcn_sched_barrier(0);
    if (kk + OQD < 32) ldo(kk + OQD, sl);
  };
  static_for16(step);
  STAMP(1, 3);
  static_for16([&](auto kc) { step(IC<16 + decltype(kc)::value>{}); });
  STAMP(1, 1);
  f32x4* red = (f32x4*)smem;
#pragma unroll
  for (int qb = 0; qb < 3; qb++)
#pragma unroll
    for (int db = 0; db < 4; db++) red[w * (OQD * 256) + (qb * 4 + db) * 64 + l] = acc[db][qb];
  __syncthreads();
  __amdgpu_buffer_rsrc_t rout = mkrsrc(rfl_ptr(out + (size_t)b * SQ * 2048 + br * 1024 + ns * 128), (SQ - 1) * 2048 + 128);
#pragma unroll
  for (int it = 0; it < 2; it++) {
    const int wv = w + 4 * it;
    if (wv < 6) {
      const int q = 8 * wv + (l >> 3), c8 = l & 7;
      const int qb = q >> 4, idx = (qb * 4 + (c8 >> 1)) * 64 + (2 * (c8 & 1)) * 16 + (q & 15);
      f32x4 s0 = {0.f, 0.f, 0.f, 0.f}, s1 = s0;
#pragma unroll
      for (int ww = 0; ww < 4; ww++) {
        s0 += red[ww * (OQD * 256) + idx];
        s1 += red[ww * (OQD * 256) + idx + 16];
      }
      u32x4 o = {pack2(s0[0], s0[1]), pack2(s0[2], s0[3]), pack2(s1[0], s1[1]), pack2(s1[2], s1[3])};
      __builtin_amdgcn_raw_buffer_store_b128(o, rout, q * 2048 + c8 * 16, 0, OUT_AUX);
    }
  }
  STAMP(1, 2);
}

extern "C" void kernel_launch(void* const* d_in, const int* in_sizes, int n_in, void* d_out, int out_size,
                              void* d_ws, size_t ws_size, hipStream_t stream) {
  const char* xA = (const char*)d_in[0]; const char* KA = (const char*)d_in[1]; const char* VA = (const char*)d_in[2];
  const char* wqA = (const char*)d_in[3]; const char* woA = (const char*)d_in[4];
  const char* xB = (const char*)d_in[5]; const char* KB = (const char*)d_in[6]; const char* VB = (const char*)d_in[7];
  const char* wqB = (const char*)d_in[8]; const char* woB = (const char*)d_in[9];
  u32x4* Obuf = (u32x4*)d_ws;
  (void)hipFuncSetAttribute((const void*)attn_kernel, hipFuncAttributeMaxDynamicSharedMemorySize, ATTN_LDS);
  (void)hipFuncSetAttribute((const void*)oproj_kernel, hipFuncAttributeMaxDynamicSharedMemorySize, OP_LDS);
  attn_kernel<<<256, 256, ATTN_LDS, stream>>>(xA, KA, VA, wqA, xB, KB, VB, wqB, woA, woB, Obuf);
  oproj_kernel<<<256, 256, OP_LDS, stream>>>(Obuf, (char*)d_out);
}
```

```cpp
#include <hip/hip_runtime.h>
#include <hip/hip_bf16.h>
#include <stdint.h>

typedef short s16x4 __attribute__((ext_vector_type(4)));
typedef float f32x4 __attribute__((ext_vector_type(4)));
typedef unsigned u32x4 __attribute__((ext_vector_type(4)));
typedef unsigned u32x2 __attribute__((ext_vector_type(2)));
typedef __bf16 bf16x8 __attribute__((ext_vector_type(8)));
typedef __bf16 bf16x2 __attribute__((ext_vector_type(2)));
typedef float f32x2 __attribute__((ext_vector_type(2)));
typedef __attribute__((address_space(3))) s16x4* lds_s16x4_p;
typedef __attribute__((address_space(3))) char* lds_char_p;

constexpr int NB = 16, SQ = 48, DM = 512, NH = 32, SK = 1024, DH = 128;
constexpr int STAGE = 49152;
constexpr int NSLOT = 3;
constexpr int NT = SK / 32;
constexpr int ATTN_LDS = NSLOT * STAGE + 16384;
constexpr float SCL2 = 0.08838834764831845f * 1.4426950408889634f;
constexpr float RESCALE_THR = 8.0f / SCL2;
constexpr int QW_SLOT = 24576, QX_SLOT = 18432;
#define KV0_STEP 13
constexpr int QNW = 4, QNX = 3;
constexpr int WPK_OFF = 1 << 20;
#define PFN 0
#define PFP 0
#define PFS0 2
#define PFDS 3
#define PFT0 5
#define PFDT 7
#define PFPOL " nt"
constexpr int pf_at(int kk) { int n = 0; for (int j = PFP; j < PFN; j++) if (PFS0 + (j - PFP) * PFDS == kk) n = j; return n; }
constexpr int QW_OFF = ATTN_LDS - QNW * QW_SLOT - QNX * QX_SLOT, QX_OFF = ATTN_LDS - QNX * QX_SLOT;
#define MAPV 1
#define NSMAP (4 * (xj & 1) + (ci & 3))
#define WUMAP ((ci * 4 + w) * 2 + ((xj >> 1) & 1))
#define ST_AUX 16
#define SW_AUX 16
#define OUT_AUX 16
#define OROT 0
#define WO_TILE 31
#define QROT (ci >> 2)

#define STAMP(k, i)

__device__ __forceinline__ unsigned pack2(float a, float b) {
  f32x2 v = {a, b};
  bf16x2 r = __builtin_convertvector(v, bf16x2);
  return __builtin_bit_cast(unsigned, r);
}
__device__ __forceinline__ f32x4 mfma16(u32x4 a, u32x4 b, f32x4 c) {
  return __builtin_amdgcn_mfma_f32_16x16x32_bf16(__builtin_bit_cast(bf16x8, a), __builtin_bit_cast(bf16x8, b), c, 0, 0, 0);
}
__device__ __forceinline__ __amdgpu_buffer_rsrc_t mkrsrc(const void* p, unsigned bytes) {
  return __builtin_amdgcn_make_buffer_rsrc((void*)p, 0, bytes, 0x00020000);
}
__device__ __forceinline__ const char* rfl_ptr(const char* p) {
  uint64_t v = (uint64_t)p;
  unsigned lo = __builtin_amdgcn_readfirstlane((unsigned)v), hi = __builtin_amdgcn_readfirstlane((unsigned)(v >> 32));
  return (const char*)(((uint64_t)hi << 32) | lo);
}
__device__ __forceinline__ u32x4 tr_frag(const char* p, int hi_off) {
  s16x4 v0 = __builtin_amdgcn_ds_read_tr16_b64_v4i16((lds_s16x4_p)(p));
  s16x4 v1 = __builtin_amdgcn_ds_read_tr16_b64_v4i16((lds_s16x4_p)(p + hi_off));
  u32x2 a0 = __builtin_bit_cast(u32x2, v0), a1 = __builtin_bit_cast(u32x2, v1);
  return u32x4{a0[0], a0[1], a1[0], a1[1]};
}

__device__ __forceinline__ void dma4(unsigned m0v, __amdgpu_buffer_rsrc_t r, unsigned soff,
                                     unsigned v0, unsigned v1, unsigned v2, unsigned v3) {
  asm volatile(
      "s_mov_b32 m0, %0\n\ts_nop 0\n\t"
      "buffer_load_dwordx4 %1, %5, %6 offen nt lds\n\t"
      "buffer_load_dwordx4 %2, %5, %6 offen offset:1024 nt lds\n\t"
      "buffer_load_dwordx4 %3, %5, %6 offen offset:2048 nt lds\n\t"
      "buffer_load_dwordx4 %4, %5, %6 offen offset:3072 nt lds"
      :: "s"(m0v), "v"(v0), "v"(v1), "v"(v2), "v"(v3), "s"(r), "s"(soff) : "memory");
}
__device__ __forceinline__ void dma4c(unsigned m0v, __amdgpu_buffer_rsrc_t r, unsigned soff,
                                     unsigned v0, unsigned v1, unsigned v2, unsigned v3) {
  asm volatile(
      "s_mov_b32 m0, %0\n\ts_nop 0\n\t"
      "buffer_load_dwordx4 %1, %5, %6 offen lds\n\t"
      "buffer_load_dwordx4 %2, %5, %6 offen offset:1024 lds\n\t"
      "buffer_load_dwordx4 %3, %5, %6 offen offset:2048 lds\n\t"
      "buffer_load_dwordx4 %4, %5, %6 offen offset:3072 lds"
      :: "s"(m0v), "v"(v0), "v"(v1), "v"(v2), "v"(v3), "s"(r), "s"(soff) : "memory");
}
__device__ __forceinline__ void dma2(unsigned m0v, __amdgpu_buffer_rsrc_t r, unsigned soff, unsigned v0, unsigned v1) {
  asm volatile(
      "s_mov_b32 m0, %0\n\ts_nop 0\n\t"
      "buffer_load_dwordx4 %1, %3, %4 offen lds\n\t"
      "buffer_load_dwordx4 %2, %3, %4 offen offset:1024 lds"
      :: "s"(m0v), "v"(v0), "v"(v1), "s"(r), "s"(soff) : "memory");
}
#define WQPOL ""
__device__ __forceinline__ void dma2w(unsigned m0v, __amdgpu_buffer_rsrc_t r, unsigned soff, unsigned v0, unsigned v1) {
  asm volatile(
      "s_mov_b32 m0, %0\n\ts_nop 0\n\t"
      "buffer_load_dwordx4 %1, %3, %4 offen" WQPOL " lds\n\t"
      "buffer_load_dwordx4 %2, %3, %4 offen offset:1024" WQPOL " lds"
      :: "s"(m0v), "v"(v0), "v"(v1), "s"(r), "s"(soff) : "memory");
}
template <int N> __device__ __forceinline__ void wait_vm() {
  asm volatile("s_waitcnt vmcnt(%0) lgkmcnt(0)" :: "n"(N) : "memory");
}
template <int V> struct IC { static constexpr int value = V; };
template <class F> __device__ __forceinline__ void static_for16(F&& f) {
  f(IC<0>{}); f(IC<1>{}); f(IC<2>{}); f(IC<3>{}); f(IC<4>{}); f(IC<5>{}); f(IC<6>{}); f(IC<7>{});
  f(IC<8>{}); f(IC<9>{}); f(IC<10>{}); f(IC<11>{}); f(IC<12>{}); f(IC<13>{}); f(IC<14>{}); f(IC<15>{});
}
constexpr int qslot(int kk) { return kk % QNW; }
constexpr int qwait(int kk, int nx) {
  if (kk >= 15) return 63;
  int n = 0, wend[16] = {}, xend[8] = {};
  for (int i = 0; i < QNW; i++) {
    if (i != 1) { n += nx; xend[(i + 1) / 2] = n; }
    n += 6; wend[i] = n;
  }
  n += PFP < PFN ? PFP : PFN;
  for (int k = 0; k < kk; k++) {
    if (k + QNW < 16) { n += 6; wend[k + QNW] = n; }
    if ((k & 1) && k < 11) { n += nx; xend[(k + 5) / 2] = n; }
    if (pf_at(k)) n += 1;
    if (k == KV0_STEP) n += 12;
  }
  const int xe = nx ? xend[(kk + 1) / 2] : 0;
  const int need = wend[kk + 1] > xe ? wend[kk + 1] : xe;
  return n - need;
}
__device__ __forceinline__ void scale_acc(f32x4& x, float alpha) {
  asm volatile("" : "+a"(x));
  x *= alpha;
  asm volatile("" : "+a"(x));
}

__global__ __launch_bounds__(256, 1) void attn_kernel(
    const char* __restrict__ xA, const char* __restrict__ KA, const char* __restrict__ VA, const char* __restrict__ wqA,
    const char* __restrict__ xB, const char* __restrict__ KB, const char* __restrict__ VB, const char* __restrict__ wqB,
    const char* __restrict__ woA, const char* __restrict__ woB, u32x4* __restrict__ Obuf) {
  extern __shared__ __attribute__((aligned(16))) char smem[];
  const int bid = blockIdx.x, xj = bid & 7, ci = bid >> 3;
  const int w = __builtin_amdgcn_readfirstlane(threadIdx.x >> 6), l = threadIdx.x & 63;
  const int r = l & 15, g = l >> 4;
  const unsigned ldsbase = (unsigned)(uintptr_t)(lds_char_p)smem;
  const int wid = bid * 4 + w; (void)wid;
  STAMP(0, 0);

  const int hM = 4 * xj + (ci & 3), bM = 2 * (ci >> 2);
  const int bG = ci >> 1, kvh = xj;
  const int hG = 4 * kvh + 2 * (ci & 1);
  const size_t U = (size_t)SK * DH * 2;

  __amdgpu_buffer_rsrc_t drs[3];
  unsigned dvo[3][4], dso[3], dm0[3];
#pragma unroll
  for (int e = 0; e < 3; e++) {
    const int G = 3 * w + e, st = G >> 2, isV = (G >> 1) & 1, hf = G & 1;
    const char* base = st == 2 ? (isV ? VA : KA) + (size_t)(bG * 8 + kvh) * U
                               : (isV ? VB : KB) + (size_t)((bM + st) * NH + hM) * U;
    drs[e] = mkrsrc(rfl_ptr(base), (unsigned)U);
    dso[e] = hf * 4096;
    dm0[e] = ldsbase + G * 4096;
#pragma unroll
    for (int i = 0; i < 4; i++) {
      const int sw = isV ? ((4 * (i & 1) + g) << 1) : (4 * g + i);
      dvo[e][i] = g * 256 + ((r ^ sw) << 4);
    }
  }
  auto issue_group = [&](int e, int t, int slot) {
    dma4(dm0[e] + slot * STAGE, drs[e], dso[e] + t * 8192, dvo[e][0], dvo[e][1], dvo[e][2], dvo[e][3]);
  };
  const int tb = w < 2 ? bM + w : bG, th = w < 2 ? hM : hG + (w - 2);
  u32x4 qf[3][4];
  unsigned pfd = 0;
  auto touch = [&](int j) {
    const unsigned tv = (l & 31) * 128 + (l >> 5) * (PFDT * 8192);
    asm volatile("buffer_load_dword %0, %1, %2, %3 offen" PFPOL
                 : "+v"(pfd) : "v"(tv), "s"(drs[j % 3]), "s"(dso[j % 3] + (PFT0 + 2 * (j / 3) * PFDT) * 8192) : "memory");
  };
  {
    __amdgpu_buffer_rsrc_t rwM = mkrsrc(rfl_ptr(wqB + (size_t)hM * DH * 2), (DM - 1) * NH * DH * 2 + DH * 2);
    __amdgpu_buffer_rsrc_t rwG = mkrsrc(rfl_ptr(wqA + (size_t)hG * DH * 2), (DM - 1) * NH * DH * 2 + 2 * DH * 2);
    const unsigned wv0 = (8 * w + g) * (NH * DH * 2) + ((r ^ (g | ((w & 1) << 3))) << 4);
    const unsigned wv1 = wv0 + 4 * (NH * DH * 2) - 1024;
    __amdgpu_buffer_rsrc_t rx = mkrsrc(rfl_ptr((w < 2 ? xB : xA) + (size_t)tb * SQ * DM * 2), SQ * DM * 2);
    const unsigned xv0 = (l >> 3) * (DM * 2) + (((l & 7) ^ g) << 4);
    const unsigned xv1 = (l >> 3) * (DM * 2) + (((l & 7) ^ (4 + g)) << 4) + 8 * (DM * 2) - 1024;
    const int rot = 2 * QROT;
    auto ldw = [&](int kk, int i) {
      const unsigned so = ((kk + rot) & 15) * 32 * (NH * DH * 2), m = ldsbase + QW_OFF + qslot(kk) * QW_SLOT + w * 2048;
      dma2w(m + i * 8192, i == 0 ? rwM : rwG, so + (i == 2 ? DH * 2 : 0), wv0, wv1);
    };
    auto ldx = [&](int j, int i) {
      const unsigned so = ((2 * j + rot) & 15) * 64, m = ldsbase + QX_OFF + (j % QNX) * QX_SLOT + w * 6144;
      if (w < 3) dma2(m + i * 2048, rx, so + i * 16 * (DM * 2), xv0, xv1);
    };
#pragma unroll
    for (int k = 0; k < QNW; k++) {
#pragma unroll
      for (int i = 0; i < 3; i++) if (k != 1) ldx((k + 1) / 2, i);
#pragma unroll
      for (int i = 0; i < 3; i++) ldw(k, i);
    }
#pragma unroll
    for (int j = 0; j < PFP && j < PFN; j++) touch(j);
    f32x4 qa[8][3];
#pragma unroll
    for (int i = 0; i < 8; i++)
#pragma unroll
      for (int qb = 0; qb < 3; qb++) qa[i][qb] = f32x4{0.f, 0.f, 0.f, 0.f};
    const unsigned abase = QW_OFF + (w < 2 ? 0 : w - 1) * 8192 + (8 * g + (r >> 2)) * 256 + ((r & 2) << 2);
    const unsigned asw = (r >> 2) | ((g & 1) << 3), ach = 4 * (r & 3);
    const unsigned xbase = QX_OFF + (w < 2 ? w : 2) * 6144 + r * 128 + ((g ^ (r >> 1)) << 4);
    u32x4 af[2][8], xf[2][3];
    auto rdx = [&](int kk, int qb) {
      xf[kk & 1][qb] = *(const u32x4*)(smem + ((kk >> 1) % QNX) * QX_SLOT + ((xbase + qb * 2048) ^ ((kk & 1) << 6)));
    };
    auto rda = [&](int kk, int i) {
      af[kk & 1][i] = tr_frag(smem + qslot(kk) * QW_SLOT + ((abase + (((ach + (i >> 1)) ^ asw) << 4)) ^ ((i & 1) << 3)), 1024);
    };
    if (w < 3) wait_vm<qwait(-1, 6)>(); else wait_vm<qwait(-1, 0)>();
    asm volatile("s_barrier" ::: "memory");
#pragma unroll
    for (int qb = 0; qb < 3; qb++) rdx(0, qb);
#pragma unroll
    for (int i = 0; i < 8; i++) rda(0, i);
    static_for16([&](auto kc) {
      constexpr int kk = decltype(kc)::value;
      if (w < 3) wait_vm<qwait(kk, 6)>(); else wait_vm<qwait(kk, 0)>();
      asm volatile("s_barrier" ::: "memory");
#pragma unroll
      for (int i = 0; i < 8; i++) {
        if (kk < 15) {
          if (i < 3) rdx(kk + 1, i);
          rda(kk + 1, i);
        }
#pragma unroll
        for (int qb = 0; qb < 3; qb++) qa[i][qb] = mfma16(af[kk & 1][i], xf[kk & 1][qb], qa[i][qb]);
        if (kk + QNW < 16 && i < 3) ldw(kk + QNW, i);
        if ((kk & 1) && kk < 11 && i >= 3 && i < 6) ldx((kk + 5) / 2, i - 3);
        if (pf_at(kk) && i == 6) touch(pf_at(kk));
        if (kk == KV0_STEP && i < 3) issue_group(i, 0, 0);
        if (kk == 15 && i >= 5) issue_group(i - 5, 1, 1);
        __builtin_amdgcn_sched_barrier(0);
      }
    });
    const bool odd = g >> 1;
#pragma unroll
    for (int qb = 0; qb < 3; qb++)
#pragma unroll
      for (int s = 0; s < 4; s++) {
        const unsigned a0 = pack2(qa[2 * s][qb][0], qa[2 * s][qb][1]), a1 = pack2(qa[2 * s][qb][2], qa[2 * s][qb][3]);
        const unsigned b0 = pack2(qa[2 * s + 1][qb][0], qa[2 * s + 1][qb][1]), b1 = pack2(qa[2 * s + 1][qb][2], qa[2 * s + 1][qb][3]);
        qf[qb][s] = u32x4{odd ? b0 : a0, odd ? b1 : a1, odd ? a0 : b0, odd ? a1 : b1};
      }
  }
#pragma unroll
  for (int e = 0; e < 3; e++) issue_group(e, 2, 2);
  STAMP(0, 1);

  const int strm = w < 2 ? w : 2;
  unsigned kaddr[4], vaddr0;
#pragma unroll
  for (int s = 0; s < 4; s++) kaddr[s] = strm * 16384 + r * 256 + (((4 * g + s) ^ (4 * (r & 3) + (r >> 2))) << 4);
  {
    const int row = 4 * g + (r >> 2), p = r & 3;
    vaddr0 = strm * 16384 + 8192 + row * 256 + ((((p >> 1)) ^ ((row & 7) << 1)) << 4) + (p & 1) * 8;
  }
  f32x4 oa[8][3];
#pragma unroll
  for (int i = 0; i < 8; i++)
#pragma unroll
    for (int qb = 0; qb < 3; qb++) oa[i][qb] = f32x4{0.f, 0.f, 0.f, 0.f};
  float mrow[3] = {-INFINITY, -INFINITY, -INFINITY}, lsum[3] = {0.f, 0.f, 0.f};

  asm volatile("s_waitcnt vmcnt(24) lgkmcnt(0)\n\ts_barrier" :: "v"(pfd) : "memory");
  int slot = 0;
  const int wu = WUMAP;
  for (int t = 0; t < NT; t++) {
    const bool refill = t + 3 < NT;
    const char* sb = smem + slot * STAGE;
    u32x4 kf[2][4], vf[8];
#pragma unroll
    for (int s = 0; s < 4; s++)
#pragma unroll
      for (int kb = 0; kb < 2; kb++) kf[kb][s] = *(const u32x4*)(sb + kaddr[s] + kb * 4096);
    __builtin_amdgcn_sched_barrier(0);
    f32x4 sa[2][3];
#pragma unroll
    for (int s = 0; s < 4; s++) {
#pragma unroll
      for (int kb = 0; kb < 2; kb++)
#pragma unroll
        for (int qb = 0; qb < 3; qb++)
          sa[kb][qb] = mfma16(kf[kb][s], qf[qb][s], s == 0 ? f32x4{0.f, 0.f, 0.f, 0.f} : sa[kb][qb]);
      vf[2 * s] = tr_frag(sb + (vaddr0 ^ ((2 * s) << 5)), 4096);
      vf[2 * s + 1] = tr_frag(sb + (vaddr0 ^ ((2 * s + 1) << 5)), 4096);
      __builtin_amdgcn_sched_barrier(0);
    }
    asm volatile("s_waitcnt lgkmcnt(0)\n\ts_barrier" ::: "memory");
    if (refill) { issue_group(0, t + 3, slot); issue_group(1, t + 3, slot); }
    if (t == NT - 3) {
      __amdgpu_buffer_rsrc_t rp = mkrsrc(rfl_ptr((xj & 4 ? woB : woA) + (size_t)wu * 16384 + (xj & 1) * 512 - 3072), 18944);
      const unsigned pv = 3072 + (l >> 3) * 1024 + (l & 7) * 16;
      dma4c(ldsbase + 2 * STAGE + w * 8192, rp, 0, pv, pv - 896, pv - 1792, pv - 2688);
      dma4c(ldsbase + 2 * STAGE + w * 8192 + 4096, rp, 8192, pv, pv - 896, pv - 1792, pv - 2688);
    }
    float mx[3];
    bool need = false;
#pragma unroll
    for (int qb = 0; qb < 3; qb++) {
      float m0 = __builtin_fmaxf(__builtin_fmaxf(sa[0][qb][0], sa[0][qb][1]), sa[0][qb][2]);
      m0 = __builtin_fmaxf(__builtin_fmaxf(m0, sa[0][qb][3]), sa[1][qb][0]);
      m0 = __builtin_fmaxf(__builtin_fmaxf(m0, sa[1][qb][1]), sa[1][qb][2]);
      mx[qb] = __builtin_fmaxf(m0, sa[1][qb][3]);
      need |= mx[qb] > mrow[qb] + RESCALE_THR;
    }
    if (__builtin_expect(__builtin_amdgcn_ballot_w64(need) != 0, 0)) {
#pragma unroll
      for (int qb = 0; qb < 3; qb++) {
        float m = mx[qb];
        m = fmaxf(m, __shfl_xor(m, 16));
        m = fmaxf(m, __shfl_xor(m, 32));
        m = fmaxf(m, mrow[qb]);
        const float alpha = __builtin_amdgcn_exp2f((mrow[qb] - m) * SCL2);
        mrow[qb] = m;
        lsum[qb] *= alpha;
#pragma unroll
        for (int i = 0; i < 8; i++) scale_acc(oa[i][qb], alpha);
      }
    }
    u32x4 pf[3];
#pragma unroll
    for (int qb = 0; qb < 3; qb++) {
      const float mc = mrow[qb] * SCL2;
      float p[8];
#pragma unroll
      for (int kb = 0; kb < 2; kb++)
#pragma unroll
        for (int e = 0; e < 4; e++) p[kb * 4 + e] = __builtin_amdgcn_exp2f(__builtin_fmaf(sa[kb][qb][e], SCL2, -mc));
      lsum[qb] += ((p[0] + p[1]) + (p[2] + p[3])) + ((p[4] + p[5]) + (p[6] + p[7]));
#pragma unroll
      for (int e = 0; e < 4; e++) pf[qb][e] = pack2(p[2 * e], p[2 * e + 1]);
    }
    if (refill) issue_group(2, t + 3, slot);
#pragma unroll
    for (int qb = 0; qb < 3; qb++)
#pragma unroll
      for (int db = 0; db < 8; db++) oa[db][qb] = mfma16(vf[db], pf[qb], oa[db][qb]);
    slot = slot == 2 ? 0 : slot + 1;
    if (t >= NT - 2) asm volatile("s_waitcnt vmcnt(0) lgkmcnt(0)\n\ts_barrier" ::: "memory");
    else if (t == NT - 3) asm volatile("s_waitcnt vmcnt(20) lgkmcnt(0)\n\ts_barrier" ::: "memory");
    else asm volatile("s_waitcnt vmcnt(24) lgkmcnt(0)\n\ts_barrier" ::: "memory");
  }
  STAMP(0, 10);

  __amdgpu_buffer_rsrc_t rsw = mkrsrc(rfl_ptr((const char*)(Obuf + WPK_OFF + ((xj >> 2) * 8 + (xj & 1) * 4) * 32768 + wu * 128)),
                                      3 * 524288 + 2048);
#pragma unroll
  for (int i = 0; i < 8; i++)
    __builtin_amdgcn_raw_buffer_store_b128(*(const u32x4*)(smem + 2 * STAGE + w * 8192 + i * 1024 + l * 16), rsw, l * 16,
                                           (i & 3) * 524288 + (i >> 2) * 1024, SW_AUX);
  const int task = ((w < 2 ? 1 : 0) * NB + tb) * NH + th;
  __amdgpu_buffer_rsrc_t rso = mkrsrc(rfl_ptr((const char*)(Obuf + (size_t)task * 12 * 64)), 12 * 1024);
#pragma unroll
  for (int qb = 0; qb < 3; qb++) {
    float s = lsum[qb];
    s += __shfl_xor(s, 16);
    s += __shfl_xor(s, 32);
    const float inv = 1.0f / s;
#pragma unroll
    for (int u = 0; u < 4; u++) {
      u32x4 o;
      o[0] = pack2(oa[2 * u][qb][0] * inv, oa[2 * u][qb][1] * inv);
      o[1] = pack2(oa[2 * u][qb][2] * inv, oa[2 * u][qb][3] * inv);
      o[2] = pack2(oa[2 * u + 1][qb][0] * inv, oa[2 * u + 1][qb][1] * inv);
      o[3] = pack2(oa[2 * u + 1][qb][2] * inv, oa[2 * u + 1][qb][3] * inv);
      __builtin_amdgcn_raw_buffer_store_b128(o, rso, l * 16, (u * 3 + qb) * 1024, ST_AUX);
    }
  }
  STAMP(0, 11);
}

constexpr int OQD = 8;
constexpr int OP_LDS = 4 * OQD * 4096;
__global__ __launch_bounds__(256, 1) void oproj_kernel(const u32x4* __restrict__ Obuf, char* __restrict__ out) {
  extern __shared__ __attribute__((aligned(16))) char smem[];
  const int bid = blockIdx.x, xj = bid & 7, ci = bid >> 3;
  const int br = xj >> 2, b = 8 * ((xj >> 1) & 1) + (ci >> 2), ns = NSMAP;
  const int w = __builtin_amdgcn_readfirstlane(threadIdx.x >> 6), l = threadIdx.x & 63;
  const int r = l & 15, g = l >> 4;
  const int wid = bid * 4 + w; (void)wid;
  STAMP(1, 0);
  const unsigned ring = (unsigned)(uintptr_t)(lds_char_p)smem + w * (OQD * 4096);
  const char* wp = rfl_ptr((const char*)(Obuf + WPK_OFF + (br * 8 + ns) * 32768 + w * 8192));
  __amdgpu_buffer_rsrc_t rw = mkrsrc(wp, 1024 * 128);
  const char* op = rfl_ptr((const char*)(Obuf + (size_t)((br * NB + b) * NH + w * 8) * 12 * 64));
  __amdgpu_buffer_rsrc_t ro = mkrsrc(op, 8 * 12 * 1024);
  const unsigned wvo = (l >> 3) * 128 + (((l & 7) ^ (((l >> 4) & 3) << 1)) << 4);
  const unsigned ovo = l * 16;
  u32x4 ob[OQD][3];
  f32x4 acc[4][3];
#pragma unroll
  for (int i = 0; i < 4; i++)
#pragma unroll
    for (int qb = 0; qb < 3; qb++) acc[i][qb] = f32x4{0.f, 0.f, 0.f, 0.f};
  const int rot = OROT;
  auto ldw = [&](int kk, int sl) {
    asm volatile(
        "s_mov_b32 m0, %0\n\ts_nop 0\n\t"
        "buffer_load_dwordx4 %1, %5, %6 offen lds\n\t"
        "buffer_load_dwordx4 %2, %5, %6 offen offset:1024 lds\n\t"
        "buffer_load_dwordx4 %3, %5, %6 offen offset:2048 lds\n\t"
        "buffer_load_dwordx4 %4, %5, %6 offen offset:3072 lds"
        :: "s"(ring + sl * 4096), "v"(wvo), "v"(wvo), "v"(wvo), "v"(wvo), "s"(rw),
           "s"(((kk + rot) & 31) * 4096)
        : "memory");
  };
  auto ldo = [&](int kk, int sl) {
    asm volatile(
        "buffer_load_dwordx4 %0, %3, %4, %5 offen\n\t"
        "buffer_load_dwordx4 %1, %3, %4, %5 offen offset:1024\n\t"
        "buffer_load_dwordx4 %2, %3, %4, %5 offen offset:2048"
        : "=&v"(ob[sl][0]), "=&v"(ob[sl][1]), "=&v"(ob[sl][2])
        : "v"(ovo), "s"(ro), "s"(((kk + rot) & 31) * 3072)
        : "memory");
  };
  const int row = 4 * g + (r >> 2), p = r & 3;
  const unsigned aoff = w * (OQD * 4096) + row * 128 + (((p >> 1) ^ (((row >> 1) & 3) << 1)) << 4) + (p & 1) * 8;
#pragma unroll
  for (int kk = 0; kk < OQD; kk++) { ldw(kk, kk); ldo(kk, kk); }
  u32x4 af[2][4];
  asm volatile("s_waitcnt vmcnt(%0)" :: "n"(7 * OQD - 4) : "memory");
#pragma unroll
  for (int db = 0; db < 4; db++) af[0][db] = tr_frag(smem + (aoff ^ (db << 5)), 2048);
  auto step = [&](auto kc) {
    constexpr int kk = decltype(kc)::value, sl = kk % OQD;
    constexpr int last = kk + OQD - 1 < 31 ? kk + OQD - 1 : 31;
    constexpr int allow = kk < 31 ? 7 * (last - kk - 1) + 3 : 0;
    asm volatile("s_waitcnt vmcnt(%3) lgkmcnt(0)" : "+v"(ob[sl][0]), "+v"(ob[sl][1]), "+v"(ob[sl][2]) : "n"(allow) : "memory");
    if (kk < 31) {
#pragma unroll
      for (int db = 0; db < 4; db++) af[(kk + 1) & 1][db] = tr_frag(smem + ((kk + 1) % OQD) * 4096 + (aoff ^ (db << 5)), 2048);
    }
#pragma unroll
    for (int db = 0; db < 2; db++)
#pragma unroll
      for (int qb = 0; qb < 3; qb++) acc[db][qb] = mfma16(af[kk & 1][db], ob[sl][qb], acc[db][qb]);
    __builtin_amdgcn_sched_barrier(0);
    if (kk + OQD < 32) ldw(kk + OQD, sl);
#pragma unroll
    for (int db = 2; db < 4; db++)
#pragma unroll
      for (int qb = 0; qb < 3; qb++) acc[db][qb] = mfma16(af[kk & 1][db], ob[sl][qb], acc[db][qb]);
    __builtin_amdgcn_sched_barrier(0);
    if (kk + OQD < 32) ldo(kk + OQD, sl);
  };
  static_for16(step);
  STAMP(1, 3);
  static_for16([&](auto kc) { step(IC<16 + decltype(kc)::value>{}); });
  STAMP(1, 1);
  f32x4* red = (f32x4*)smem;
#pragma unroll
  for (int qb = 0; qb < 3; qb++)
#pragma unroll
    for (int db = 0; db < 4; db++) red[w * (OQD * 256) + (qb * 4 + db) * 64 + l] = acc[db][qb];
  __syncthreads();
  __amdgpu_buffer_rsrc_t rout = mkrsrc(rfl_ptr(out + (size_t)b * SQ * 2048 + br * 1024 + ns * 128), (SQ - 1) * 2048 + 128);
#pragma unroll
  for (int it = 0; it < 2; it++) {
    const int wv = w + 4 * it;
    if (wv < 6) {
      const int q = 8 * wv + (l >> 3), c8 = l & 7;
      const int qb = q >> 4, idx = (qb * 4 + (c8 >> 1)) * 64 + (2 * (c8 & 1)) * 16 + (q & 15);
      f32x4 s0 = {0.f, 0.f, 0.f, 0.f}, s1 = s0;
#pragma unroll
      for (int ww = 0; ww < 4; ww++) {
        s0 += red[ww * (OQD * 256) + idx];
        s1 += red[ww * (OQD * 256) + idx + 16];
      }
      u32x4 o = {pack2(s0[0], s0[1]), pack2(s0[2], s0[3]), pack2(s1[0], s1[1]), pack2(s1[2], s1[3])};
      __builtin_amdgcn_raw_buffer_store_b128(o, rout, q * 2048 + c8 * 16, 0, OUT_AUX);
    }
  }
  STAMP(1, 2);
}

extern "C" void kernel_launch(void* const* d_in, const int* in_sizes, int n_in, void* d_out, int out_size,
                              void* d_ws, size_t ws_size, hipStream_t stream) {
  const char* xA = (const char*)d_in[0]; const char* KA = (const char*)d_in[1]; const char* VA = (const char*)d_in[2];
  const char* wqA = (const char*)d_in[3]; const char* woA = (const char*)d_in[4];
  const char* xB = (const char*)d_in[5]; const char* KB = (const char*)d_in[6]; const char* VB = (const char*)d_in[7];
  const char* wqB = (const char*)d_in[8]; const char* woB = (const char*)d_in[9];
  u32x4* Obuf = (u32x4*)d_ws;
  (void)hipFuncSetAttribute((const void*)attn_kernel, hipFuncAttributeMaxDynamicSharedMemorySize, ATTN_LDS);
  (void)hipFuncSetAttribute((const void*)oproj_kernel, hipFuncAttributeMaxDynamicSharedMemorySize, OP_LDS);
  attn_kernel<<<256, 256, ATTN_LDS, stream>>>(xA, KA, VA, wqA, xB, KB, VB, wqB, woA, woB, Obuf);
  oproj_kernel<<<256, 256, OP_LDS, stream>>>(Obuf, (char*)d_out);
}
```

```cpp
#include <hip/hip_runtime.h>
#include <hip/hip_bf16.h>
#include <stdint.h>

typedef short s16x4 __attribute__((ext_vector_type(4)));
typedef float f32x4 __attribute__((ext_vector_type(4)));
typedef unsigned u32x4 __attribute__((ext_vector_type(4)));
typedef unsigned u32x2 __attribute__((ext_vector_type(2)));
typedef __bf16 bf16x8 __attribute__((ext_vector_type(8)));
typedef __bf16 bf16x2 __attribute__((ext_vector_type(2)));
typedef float f32x2 __attribute__((ext_vector_type(2)));
typedef __attribute__((address_space(3))) s16x4* lds_s16x4_p;
typedef __attribute__((address_space(3))) char* lds_char_p;

constexpr int NB = 16, SQ = 48, DM = 512, NH = 32, SK = 1024, DH = 128;
constexpr int STAGE = 49152;
constexpr int NSLOT = 3;
constexpr int NT = SK / 32;
constexpr int ATTN_LDS = NSLOT * STAGE + 16384;
constexpr float SCL2 = 0.08838834764831845f * 1.4426950408889634f;
constexpr float RESCALE_THR = 8.0f / SCL2;
constexpr int QW_SLOT = 24576, QX_SLOT = 18432;
constexpr int KV0_STEP = 13;
constexpr int WT = 16;
constexpr int QNW = 4, QNX = 3;
constexpr int WPK_OFF = 1 << 20;
constexpr int QW_OFF = ATTN_LDS - QNW * QW_SLOT - QNX * QX_SLOT, QX_OFF = ATTN_LDS - QNX * QX_SLOT;


__device__ __forceinline__ unsigned pack2(float a, float b) {
  f32x2 v = {a, b};
  bf16x2 r = __builtin_convertvector(v, bf16x2);
  return __builtin_bit_cast(unsigned, r);
}
__device__ __forceinline__ f32x4 mfma16(u32x4 a, u32x4 b, f32x4 c) {
  return __builtin_amdgcn_mfma_f32_16x16x32_bf16(__builtin_bit_cast(bf16x8, a), __builtin_bit_cast(bf16x8, b), c, 0, 0, 0);
}
__device__ __forceinline__ __amdgpu_buffer_rsrc_t mkrsrc(const void* p, unsigned bytes) {
  return __builtin_amdgcn_make_buffer_rsrc((void*)p, 0, bytes, 0x00020000);
}
__device__ __forceinline__ const char* rfl_ptr(const char* p) {
  uint64_t v = (uint64_t)p;
  unsigned lo = __builtin_amdgcn_readfirstlane((unsigned)v), hi = __builtin_amdgcn_readfirstlane((unsigned)(v >> 32));
  return (const char*)(((uint64_t)hi << 32) | lo);
}
__device__ __forceinline__ u32x4 tr_frag(const char* p, int hi_off) {
  s16x4 v0 = __builtin_amdgcn_ds_read_tr16_b64_v4i16((lds_s16x4_p)(p));
  s16x4 v1 = __builtin_amdgcn_ds_read_tr16_b64_v4i16((lds_s16x4_p)(p + hi_off));
  u32x2 a0 = __builtin_bit_cast(u32x2, v0), a1 = __builtin_bit_cast(u32x2, v1);
  return u32x4{a0[0], a0[1], a1[0], a1[1]};
}

__device__ __forceinline__ void dma4(unsigned m0v, __amdgpu_buffer_rsrc_t r, unsigned soff,
                                     unsigned v0, unsigned v1, unsigned v2, unsigned v3) {
  asm volatile(
      "s_mov_b32 m0, %0\n\ts_nop 0\n\t"
      "buffer_load_dwordx4 %1, %5, %6 offen nt lds\n\t"
      "buffer_load_dwordx4 %2, %5, %6 offen offset:1024 nt lds\n\t"
      "buffer_load_dwordx4 %3, %5, %6 offen offset:2048 nt lds\n\t"
      "buffer_load_dwordx4 %4, %5, %6 offen offset:3072 nt lds"
      :: "s"(m0v), "v"(v0), "v"(v1), "v"(v2), "v"(v3), "s"(r), "s"(soff) : "memory");
}
__device__ __forceinline__ void dma2(unsigned m0v, __amdgpu_buffer_rsrc_t r, unsigned soff, unsigned v0, unsigned v1) {
  asm volatile(
      "s_mov_b32 m0, %0\n\ts_nop 0\n\t"
      "buffer_load_dwordx4 %1, %3, %4 offen lds\n\t"
      "buffer_load_dwordx4 %2, %3, %4 offen offset:1024 lds"
      :: "s"(m0v), "v"(v0), "v"(v1), "s"(r), "s"(soff) : "memory");
}
template <int N> __device__ __forceinline__ void wait_vm() {
  asm volatile("s_waitcnt vmcnt(%0) lgkmcnt(0)" :: "n"(N) : "memory");
}
template <int V> struct IC { static constexpr int value = V; };
template <class F> __device__ __forceinline__ void static_for16(F&& f) {
  f(IC<0>{}); f(IC<1>{}); f(IC<2>{}); f(IC<3>{}); f(IC<4>{}); f(IC<5>{}); f(IC<6>{}); f(IC<7>{});
  f(IC<8>{}); f(IC<9>{}); f(IC<10>{}); f(IC<11>{}); f(IC<12>{}); f(IC<13>{}); f(IC<14>{}); f(IC<15>{});
}
constexpr int qslot(int kk) { return kk % QNW; }
constexpr int qwait(int kk, int nx) {
  if (kk >= 15) return 63;
  int n = 0, wend[16] = {}, xend[8] = {};
  for (int i = 0; i < QNW; i++) {
    if (i != 1) { n += nx; xend[(i + 1) / 2] = n; }
    n += 6; wend[i] = n;
  }
  for (int k = 0; k < kk; k++) {
    if (k + QNW < 16) { n += 6; wend[k + QNW] = n; }
    if ((k & 1) && k < 11) { n += nx; xend[(k + 5) / 2] = n; }
    if (k == KV0_STEP) n += 12;
  }
  const int xe = nx ? xend[(kk + 1) / 2] : 0;
  const int need = wend[kk + 1] > xe ? wend[kk + 1] : xe;
  return n - need;
}
__device__ __forceinline__ void scale_acc(f32x4& x, float alpha) {
  asm volatile("" : "+a"(x));
  x *= alpha;
  asm volatile("" : "+a"(x));
}

__global__ __launch_bounds__(256, 1) void attn_kernel(
    const char* __restrict__ xA, const char* __restrict__ KA, const char* __restrict__ VA, const char* __restrict__ wqA,
    const char* __restrict__ xB, const char* __restrict__ KB, const char* __restrict__ VB, const char* __restrict__ wqB,
    const char* __restrict__ woA, const char* __restrict__ woB, u32x4* __restrict__ Obuf) {
  extern __shared__ __attribute__((aligned(16))) char smem[];
  const int bid = blockIdx.x, xj = bid & 7, ci = bid >> 3;
  const int w = __builtin_amdgcn_readfirstlane(threadIdx.x >> 6), l = threadIdx.x & 63;
  const int r = l & 15, g = l >> 4;
  const unsigned ldsbase = (unsigned)(uintptr_t)(lds_char_p)smem;

  const int hM = 4 * xj + (ci & 3), bM = 2 * (ci >> 2);
  const int bG = ci >> 1, kvh = xj;
  const int hG = 4 * kvh + 2 * (ci & 1);
  const size_t U = (size_t)SK * DH * 2;

  __amdgpu_buffer_rsrc_t drs[3];
  unsigned dvo[3][4], dso[3], dm0[3];
#pragma unroll
  for (int e = 0; e < 3; e++) {
    const int G = 3 * w + e, st = G >> 2, isV = (G >> 1) & 1, hf = G & 1;
    const char* base = st == 2 ? (isV ? VA : KA) + (size_t)(bG * 8 + kvh) * U
                               : (isV ? VB : KB) + (size_t)((bM + st) * NH + hM) * U;
    drs[e] = mkrsrc(rfl_ptr(base), (unsigned)U);
    dso[e] = hf * 4096;
    dm0[e] = ldsbase + G * 4096;
#pragma unroll
    for (int i = 0; i < 4; i++) {
      const int sw = isV ? ((4 * (i & 1) + g) << 1) : (4 * g + i);
      dvo[e][i] = g * 256 + ((r ^ sw) << 4);
    }
  }
  auto issue_group = [&](int e, int t, int slot) {
    dma4(dm0[e] + slot * STAGE, drs[e], dso[e] + t * 8192, dvo[e][0], dvo[e][1], dvo[e][2], dvo[e][3]);
  };
  const int tb = w < 2 ? bM + w : bG, th = w < 2 ? hM : hG + (w - 2);
  u32x4 qf[3][4];
  {
    __amdgpu_buffer_rsrc_t rwM = mkrsrc(rfl_ptr(wqB + (size_t)hM * DH * 2), (DM - 1) * NH * DH * 2 + DH * 2);
    __amdgpu_buffer_rsrc_t rwG = mkrsrc(rfl_ptr(wqA + (size_t)hG * DH * 2), (DM - 1) * NH * DH * 2 + 2 * DH * 2);
    const unsigned wv0 = (8 * w + g) * (NH * DH * 2) + ((r ^ (g | ((w & 1) << 3))) << 4);
    const unsigned wv1 = wv0 + 4 * (NH * DH * 2) - 1024;
    __amdgpu_buffer_rsrc_t rx = mkrsrc(rfl_ptr((w < 2 ? xB : xA) + (size_t)tb * SQ * DM * 2), SQ * DM * 2);
    const unsigned xv0 = (l >> 3) * (DM * 2) + (((l & 7) ^ g) << 4);
    const unsigned xv1 = (l >> 3) * (DM * 2) + (((l & 7) ^ (4 + g)) << 4) + 8 * (DM * 2) - 1024;
    const int rot = 2 * (ci >> 2);
    auto ldw = [&](int kk, int i) {
      const unsigned so = ((kk + rot) & 15) * 32 * (NH * DH * 2), m = ldsbase + QW_OFF + qslot(kk) * QW_SLOT + w * 2048;
      dma2(m + i * 8192, i == 0 ? rwM : rwG, so + (i == 2 ? DH * 2 : 0), wv0, wv1);
    };
    auto ldx = [&](int j, int i) {
      const unsigned so = ((2 * j + rot) & 15) * 64, m = ldsbase + QX_OFF + (j % QNX) * QX_SLOT + w * 6144;
      if (w < 3) dma2(m + i * 2048, rx, so + i * 16 * (DM * 2), xv0, xv1);
    };
#pragma unroll
    for (int k = 0; k < QNW; k++) {
#pragma unroll
      for (int i = 0; i < 3; i++) if (k != 1) ldx((k + 1) / 2, i);
#pragma unroll
      for (int i = 0; i < 3; i++) ldw(k, i);
    }
    f32x4 qa[8][3];
#pragma unroll
    for (int i = 0; i < 8; i++)
#pragma unroll
      for (int qb = 0; qb < 3; qb++) qa[i][qb] = f32x4{0.f, 0.f, 0.f, 0.f};
    const unsigned abase = QW_OFF + (w < 2 ? 0 : w - 1) * 8192 + (8 * g + (r >> 2)) * 256 + ((r & 2) << 2);
    const unsigned asw = (r >> 2) | ((g & 1) << 3), ach = 4 * (r & 3);
    const unsigned xbase = QX_OFF + (w < 2 ? w : 2) * 6144 + r * 128 + ((g ^ (r >> 1)) << 4);
    u32x4 af[2][8], xf[2][3];
    auto rdx = [&](int kk, int qb) {
      xf[kk & 1][qb] = *(const u32x4*)(smem + ((kk >> 1) % QNX) * QX_SLOT + ((xbase + qb * 2048) ^ ((kk & 1) << 6)));
    };
    auto rda = [&](int kk, int i) {
      af[kk & 1][i] = tr_frag(smem + qslot(kk) * QW_SLOT + ((abase + (((ach + (i >> 1)) ^ asw) << 4)) ^ ((i & 1) << 3)), 1024);
    };
    if (w < 3) wait_vm<qwait(-1, 6)>(); else wait_vm<qwait(-1, 0)>();
    asm volatile("s_barrier" ::: "memory");
#pragma unroll
    for (int qb = 0; qb < 3; qb++) rdx(0, qb);
#pragma unroll
    for (int i = 0; i < 8; i++) rda(0, i);
    static_for16([&](auto kc) {
      constexpr int kk = decltype(kc)::value;
      if (w < 3) wait_vm<qwait(kk, 6)>(); else wait_vm<qwait(kk, 0)>();
      asm volatile("s_barrier" ::: "memory");
#pragma unroll
      for (int i = 0; i < 8; i++) {
        if (kk < 15) {
          if (i < 3) rdx(kk + 1, i);
          rda(kk + 1, i);
        }
#pragma unroll
        for (int qb = 0; qb < 3; qb++) qa[i][qb] = mfma16(af[kk & 1][i], xf[kk & 1][qb], qa[i][qb]);
        if (kk + QNW < 16 && i < 3) ldw(kk + QNW, i);
        if ((kk & 1) && kk < 11 && i >= 3 && i < 6) ldx((kk + 5) / 2, i - 3);
        if (kk == KV0_STEP && i < 3) issue_group(i, 0, 0);
        if (kk == 15 && i >= 5) issue_group(i - 5, 1, 1);
        __builtin_amdgcn_sched_barrier(0);
      }
    });
    const bool odd = g >> 1;
#pragma unroll
    for (int qb = 0; qb < 3; qb++)
#pragma unroll
      for (int s = 0; s < 4; s++) {
        const unsigned a0 = pack2(qa[2 * s][qb][0], qa[2 * s][qb][1]), a1 = pack2(qa[2 * s][qb][2], qa[2 * s][qb][3]);
        const unsigned b0 = pack2(qa[2 * s + 1][qb][0], qa[2 * s + 1][qb][1]), b1 = pack2(qa[2 * s + 1][qb][2], qa[2 * s + 1][qb][3]);
        qf[qb][s] = u32x4{odd ? b0 : a0, odd ? b1 : a1, odd ? a0 : b0, odd ? a1 : b1};
      }
  }
#pragma unroll
  for (int e = 0; e < 3; e++) issue_group(e, 2, 2);

  const int strm = w < 2 ? w : 2;
  unsigned kaddr[4], vaddr0;
#pragma unroll
  for (int s = 0; s < 4; s++) kaddr[s] = strm * 16384 + r * 256 + (((4 * g + s) ^ (4 * (r & 3) + (r >> 2))) << 4);
  {
    const int row = 4 * g + (r >> 2), p = r & 3;
    vaddr0 = strm * 16384 + 8192 + row * 256 + ((((p >> 1)) ^ ((row & 7) << 1)) << 4) + (p & 1) * 8;
  }
  f32x4 oa[8][3];
#pragma unroll
  for (int i = 0; i < 8; i++)
#pragma unroll
    for (int qb = 0; qb < 3; qb++) oa[i][qb] = f32x4{0.f, 0.f, 0.f, 0.f};
  float mrow[3] = {-INFINITY, -INFINITY, -INFINITY}, lsum[3] = {0.f, 0.f, 0.f};

  asm volatile("s_waitcnt vmcnt(24) lgkmcnt(0)\n\ts_barrier" ::: "memory");
  int slot = 0;
  const int wu = ((ci * 4 + w) * 2 + ((xj >> 1) & 1));
  for (int t = 0; t < NT; t++) {
    const bool refill = t + 3 < NT;
    const char* sb = smem + slot * STAGE;
    u32x4 kf[2][4], vf[8];
#pragma unroll
    for (int s = 0; s < 4; s++)
#pragma unroll
      for (int kb = 0; kb < 2; kb++) kf[kb][s] = *(const u32x4*)(sb + kaddr[s] + kb * 4096);
    __builtin_amdgcn_sched_barrier(0);
    f32x4 sa[2][3];
#pragma unroll
    for (int s = 0; s < 4; s++) {
#pragma unroll
      for (int kb = 0; kb < 2; kb++)
#pragma unroll
        for (int qb = 0; qb < 3; qb++)
          sa[kb][qb] = mfma16(kf[kb][s], qf[qb][s], s == 0 ? f32x4{0.f, 0.f, 0.f, 0.f} : sa[kb][qb]);
      vf[2 * s] = tr_frag(sb + (vaddr0 ^ ((2 * s) << 5)), 4096);
      vf[2 * s + 1] = tr_frag(sb + (vaddr0 ^ ((2 * s + 1) << 5)), 4096);
      __builtin_amdgcn_sched_barrier(0);
    }
    asm volatile("s_waitcnt lgkmcnt(0)\n\ts_barrier" ::: "memory");
    if (refill) { issue_group(0, t + 3, slot); issue_group(1, t + 3, slot); }
    if (t == NT - 3) {
      __amdgpu_buffer_rsrc_t rp = mkrsrc(rfl_ptr((xj & 4 ? woB : woA) + (size_t)wu * 16384 + (xj & 1) * 512 - 3072), 18944);
      const unsigned pv = 3072 + (l >> 3) * 1024 + (l & 7) * 16;
      dma4(ldsbase + 2 * STAGE + w * 8192, rp, 0, pv, pv - 896, pv - 1792, pv - 2688);
      dma4(ldsbase + 2 * STAGE + w * 8192 + 4096, rp, 8192, pv, pv - 896, pv - 1792, pv - 2688);
    }
    float mx[3];
    bool need = false;
#pragma unroll
    for (int qb = 0; qb < 3; qb++) {
      float m0 = __builtin_fmaxf(__builtin_fmaxf(sa[0][qb][0], sa[0][qb][1]), sa[0][qb][2]);
      m0 = __builtin_fmaxf(__builtin_fmaxf(m0, sa[0][qb][3]), sa[1][qb][0]);
      m0 = __builtin_fmaxf(__builtin_fmaxf(m0, sa[1][qb][1]), sa[1][qb][2]);
      mx[qb] = __builtin_fmaxf(m0, sa[1][qb][3]);
      need |= mx[qb] > mrow[qb] + RESCALE_THR;
    }
    if (__builtin_expect(__builtin_amdgcn_ballot_w64(need) != 0, 0)) {
#pragma unroll
      for (int qb = 0; qb < 3; qb++) {
        float m = mx[qb];
        m = fmaxf(m, __shfl_xor(m, 16));
        m = fmaxf(m, __shfl_xor(m, 32));
        m = fmaxf(m, mrow[qb]);
        const float alpha = __builtin_amdgcn_exp2f((mrow[qb] - m) * SCL2);
        mrow[qb] = m;
        lsum[qb] *= alpha;
#pragma unroll
        for (int i = 0; i < 8; i++) scale_acc(oa[i][qb], alpha);
      }
    }
    u32x4 pf[3];
#pragma unroll
    for (int qb = 0; qb < 3; qb++) {
      const float mc = mrow[qb] * SCL2;
      float p[8];
#pragma unroll
      for (int kb = 0; kb < 2; kb++)
#pragma unroll
        for (int e = 0; e < 4; e++) p[kb * 4 + e] = __builtin_amdgcn_exp2f(__builtin_fmaf(sa[kb][qb][e], SCL2, -mc));
      lsum[qb] += ((p[0] + p[1]) + (p[2] + p[3])) + ((p[4] + p[5]) + (p[6] + p[7]));
#pragma unroll
      for (int e = 0; e < 4; e++) pf[qb][e] = pack2(p[2 * e], p[2 * e + 1]);
    }
    if (refill) issue_group(2, t + 3, slot);
#pragma unroll
    for (int qb = 0; qb < 3; qb++) {
#pragma unroll
      for (int db = 0; db < 8; db++) oa[db][qb] = mfma16(vf[db], pf[qb], oa[db][qb]);
    }
    slot = slot == 2 ? 0 : slot + 1;
    if (t >= NT - 2) asm volatile("s_waitcnt vmcnt(0) lgkmcnt(0)\n\ts_barrier" ::: "memory");
    else if (t == NT - 3) asm volatile("s_waitcnt vmcnt(20) lgkmcnt(0)\n\ts_barrier" ::: "memory");
    else asm volatile("s_waitcnt vmcnt(24) lgkmcnt(0)\n\ts_barrier" ::: "memory");
  }

  __amdgpu_buffer_rsrc_t rsw = mkrsrc(rfl_ptr((const char*)(Obuf + WPK_OFF + ((xj >> 2) * 8 + (xj & 1) * 4) * 32768 + wu * 128)),
                                      3 * 524288 + 2048);
#pragma unroll
  for (int i = 0; i < 8; i++)
    __builtin_amdgcn_raw_buffer_store_b128(*(const u32x4*)(smem + 2 * STAGE + w * 8192 + i * 1024 + l * 16), rsw, l * 16,
                                           (i & 3) * 524288 + (i >> 2) * 1024, WT);
  const int task = ((w < 2 ? 1 : 0) * NB + tb) * NH + th;
  __amdgpu_buffer_rsrc_t rso = mkrsrc(rfl_ptr((const char*)(Obuf + (size_t)task * 12 * 64)), 12 * 1024);
#pragma unroll
  for (int qb = 0; qb < 3; qb++) {
    float s = lsum[qb];
    s += __shfl_xor(s, 16);
    s += __shfl_xor(s, 32);
    const float inv = 1.0f / s;
#pragma unroll
    for (int u = 0; u < 4; u++) {
      u32x4 o;
      o[0] = pack2(oa[2 * u][qb][0] * inv, oa[2 * u][qb][1] * inv);
      o[1] = pack2(oa[2 * u][qb][2] * inv, oa[2 * u][qb][3] * inv);
      o[2] = pack2(oa[2 * u + 1][qb][0] * inv, oa[2 * u + 1][qb][1] * inv);
      o[3] = pack2(oa[2 * u + 1][qb][2] * inv, oa[2 * u + 1][qb][3] * inv);
      __builtin_amdgcn_raw_buffer_store_b128(o, rso, l * 16, (u * 3 + qb) * 1024, WT);
    }
  }
}

constexpr int OQD = 6;
constexpr int OP_LDS = 4 * OQD * 4096;
__global__ __launch_bounds__(256, 1) void oproj_kernel(const u32x4* __restrict__ Obuf, char* __restrict__ out) {
  extern __shared__ __attribute__((aligned(16))) char smem[];
  const int bid = blockIdx.x, xj = bid & 7, ci = bid >> 3;
  const int br = xj >> 2, b = 8 * ((xj >> 1) & 1) + (ci >> 2), ns = (4 * (xj & 1) + (ci & 3));
  const int w = __builtin_amdgcn_readfirstlane(threadIdx.x >> 6), l = threadIdx.x & 63;
  const int r = l & 15, g = l >> 4;
  const unsigned ring = (unsigned)(uintptr_t)(lds_char_p)smem + w * (OQD * 4096);
  const char* wp = rfl_ptr((const char*)(Obuf + WPK_OFF + (br * 8 + ns) * 32768 + w * 8192));
  __amdgpu_buffer_rsrc_t rw = mkrsrc(wp, 1024 * 128);
  const char* op = rfl_ptr((const char*)(Obuf + (size_t)((br * NB + b) * NH + w * 8) * 12 * 64));
  __amdgpu_buffer_rsrc_t ro = mkrsrc(op, 8 * 12 * 1024);
  const unsigned wvo = (l >> 3) * 128 + (((l & 7) ^ (((l >> 4) & 3) << 1)) << 4);
  const unsigned ovo = l * 16;
  u32x4 ob[OQD][3];
  f32x4 acc[4][3];
#pragma unroll
  for (int i = 0; i < 4; i++)
#pragma unroll
    for (int qb = 0; qb < 3; qb++) acc[i][qb] = f32x4{0.f, 0.f, 0.f, 0.f};
  auto ldw = [&](int kk, int sl) {
    asm volatile(
        "s_mov_b32 m0, %0\n\ts_nop 0\n\t"
        "buffer_load_dwordx4 %1, %5, %6 offen lds\n\t"
        "buffer_load_dwordx4 %2, %5, %6 offen offset:1024 lds\n\t"
        "buffer_load_dwordx4 %3, %5, %6 offen offset:2048 lds\n\t"
        "buffer_load_dwordx4 %4, %5, %6 offen offset:3072 lds"
        :: "s"(ring + sl * 4096), "v"(wvo), "v"(wvo), "v"(wvo), "v"(wvo), "s"(rw),
           "s"(kk * 4096)
        : "memory");
  };
  auto ldo = [&](int kk, int sl) {
    asm volatile(
        "buffer_load_dwordx4 %0, %3, %4, %5 offen\n\t"
        "buffer_load_dwordx4 %1, %3, %4, %5 offen offset:1024\n\t"
        "buffer_load_dwordx4 %2, %3, %4, %5 offen offset:2048"
        : "=&v"(ob[sl][0]), "=&v"(ob[sl][1]), "=&v"(ob[sl][2])
        : "v"(ovo), "s"(ro), "s"(kk * 3072)
        : "memory");
  };
  const int row = 4 * g + (r >> 2), p = r & 3;
  const unsigned aoff = w * (OQD * 4096) + row * 128 + (((p >> 1) ^ (((row >> 1) & 3) << 1)) << 4) + (p & 1) * 8;
#pragma unroll
  for (int kk = 0; kk < OQD; kk++) { ldw(kk, kk); ldo(kk, kk); }
  u32x4 af[2][4];
  asm volatile("s_waitcnt vmcnt(%0)" :: "n"(7 * OQD - 4) : "memory");
#pragma unroll
  for (int db = 0; db < 4; db++) af[0][db] = tr_frag(smem + (aoff ^ (db << 5)), 2048);
  auto step = [&](auto kc) {
    constexpr int kk = decltype(kc)::value, sl = kk % OQD;
    constexpr int last = kk + OQD - 1 < 31 ? kk + OQD - 1 : 31;
    constexpr int allow = kk < 31 ? 7 * (last - kk - 1) + 3 : 0;
    asm volatile("s_waitcnt vmcnt(%3) lgkmcnt(0)" : "+v"(ob[sl][0]), "+v"(ob[sl][1]), "+v"(ob[sl][2]) : "n"(allow) : "memory");
    if (kk < 31) {
#pragma unroll
      for (int db = 0; db < 4; db++) af[(kk + 1) & 1][db] = tr_frag(smem + ((kk + 1) % OQD) * 4096 + (aoff ^ (db << 5)), 2048);
    }
#pragma unroll
    for (int db = 0; db < 2; db++)
#pragma unroll
      for (int qb = 0; qb < 3; qb++) acc[db][qb] = mfma16(af[kk & 1][db], ob[sl][qb], acc[db][qb]);
    __builtin_amdgcn_sched_barrier(0);
    if (kk + OQD < 32) ldw(kk + OQD, sl);
#pragma unroll
    for (int db = 2; db < 4; db++)
#pragma unroll
      for (int qb = 0; qb < 3; qb++) acc[db][qb] = mfma16(af[kk & 1][db], ob[sl][qb], acc[db][qb]);
    __builtin_amdgcn_sched_barrier(0);
    if (kk + OQD < 32) ldo(kk + OQD, sl);
  };
  static_for16(step);
  static_for16([&](auto kc) { step(IC<16 + decltype(kc)::value>{}); });
  f32x4* red = (f32x4*)smem;
#pragma unroll
  for (int qb = 0; qb < 3; qb++)
#pragma unroll
    for (int db = 0; db < 4; db++) red[w * (OQD * 256) + (qb * 4 + db) * 64 + l] = acc[db][qb];
  __syncthreads();
  __amdgpu_buffer_rsrc_t rout = mkrsrc(rfl_ptr(out + (size_t)b * SQ * 2048 + br * 1024 + ns * 128), (SQ - 1) * 2048 + 128);
#pragma unroll
  for (int it = 0; it < 2; it++) {
    const int wv = w + 4 * it;
    if (wv < 6) {
      const int q = 8 * wv + (l >> 3), c8 = l & 7;
      const int qb = q >> 4, idx = (qb * 4 + (c8 >> 1)) * 64 + (2 * (c8 & 1)) * 16 + (q & 15);
      f32x4 s0 = {0.f, 0.f, 0.f, 0.f}, s1 = s0;
#pragma unroll
      for (int ww = 0; ww < 4; ww++) {
        s0 += red[ww * (OQD * 256) + idx];
        s1 += red[ww * (OQD * 256) + idx + 16];
      }
      u32x4 o = {pack2(s0[0], s0[1]), pack2(s0[2], s0[3]), pack2(s1[0], s1[1]), pack2(s1[2], s1[3])};
      __builtin_amdgcn_raw_buffer_store_b128(o, rout, q * 2048 + c8 * 16, 0, WT);
    }
  }
}

extern "C" void kernel_launch(void* const* d_in, const int* in_sizes, int n_in, void* d_out, int out_size,
                              void* d_ws, size_t ws_size, hipStream_t stream) {
  const char* xA = (const char*)d_in[0]; const char* KA = (const char*)d_in[1]; const char* VA = (const char*)d_in[2];
  const char* wqA = (const char*)d_in[3]; const char* woA = (const char*)d_in[4];
  const char* xB = (const char*)d_in[5]; const char* KB = (const char*)d_in[6]; const char* VB = (const char*)d_in[7];
  const char* wqB = (const char*)d_in[8]; const char* woB = (const char*)d_in[9];
  u32x4* Obuf = (u32x4*)d_ws;
  (void)hipFuncSetAttribute((const void*)attn_kernel, hipFuncAttributeMaxDynamicSharedMemorySize, ATTN_LDS);
  (void)hipFuncSetAttribute((const void*)oproj_kernel, hipFuncAttributeMaxDynamicSharedMemorySize, OP_LDS);
  attn_kernel<<<256, 256, ATTN_LDS, stream>>>(xA, KA, VA, wqA, xB, KB, VB, wqB, woA, woB, Obuf);
  oproj_kernel<<<256, 256, OP_LDS, stream>>>(Obuf, (char*)d_out);
}
```

```cpp
#include <hip/hip_runtime.h>
#include <hip/hip_bf16.h>
#include <stdint.h>

typedef short s16x4 __attribute__((ext_vector_type(4)));
typedef float f32x4 __attribute__((ext_vector_type(4)));
typedef unsigned u32x4 __attribute__((ext_vector_type(4)));
typedef unsigned u32x2 __attribute__((ext_vector_type(2)));
typedef __bf16 bf16x8 __attribute__((ext_vector_type(8)));
typedef __bf16 bf16x2 __attribute__((ext_vector_type(2)));
typedef float f32x2 __attribute__((ext_vector_type(2)));
typedef __attribute__((address_space(3))) s16x4* lds_s16x4_p;
typedef __attribute__((address_space(3))) char* lds_char_p;

constexpr int NB = 16, SQ = 48, DM = 512, NH = 32, SK = 1024, DH = 128;
constexpr int STAGE = 49152;
constexpr int NSLOT = 3;
constexpr int NT = SK / 32;
constexpr int ATTN_LDS = NSLOT * STAGE + 16384;
constexpr float SCL2 = 0.08838834764831845f * 1.4426950408889634f;
constexpr float RESCALE_THR = 8.0f / SCL2;
constexpr int QW_SLOT = 24576, QX_SLOT = 18432;
constexpr int KV0_STEP = 13;
constexpr int WT = 16;
constexpr int QNW = 4, QNX = 3;
constexpr int WPK_OFF = 1 << 20;
constexpr int QW_OFF = ATTN_LDS - QNW * QW_SLOT - QNX * QX_SLOT, QX_OFF = ATTN_LDS - QNX * QX_SLOT;


__device__ __forceinline__ unsigned pack2(float a, float b) {
  f32x2 v = {a, b};
  bf16x2 r = __builtin_convertvector(v, bf16x2);
  return __builtin_bit_cast(unsigned, r);
}
__device__ __forceinline__ f32x4 mfma16(u32x4 a, u32x4 b, f32x4 c) {
  return __builtin_amdgcn_mfma_f32_16x16x32_bf16(__builtin_bit_cast(bf16x8, a), __builtin_bit_cast(bf16x8, b), c, 0, 0, 0);
}
__device__ __forceinline__ __amdgpu_buffer_rsrc_t mkrsrc(const void* p, unsigned bytes) {
  return __builtin_amdgcn_make_buffer_rsrc((void*)p, 0, bytes, 0x00020000);
}
__device__ __forceinline__ const char* rfl_ptr(const char* p) {
  uint64_t v = (uint64_t)p;
  unsigned lo = __builtin_amdgcn_readfirstlane((unsigned)v), hi = __builtin_amdgcn_readfirstlane((unsigned)(v >> 32));
  return (const char*)(((uint64_t)hi << 32) | lo);
}
__device__ __forceinline__ u32x4 tr_frag(const char* p, int hi_off) {
  s16x4 v0 = __builtin_amdgcn_ds_read_tr16_b64_v4i16((lds_s16x4_p)(p));
  s16x4 v1 = __builtin_amdgcn_ds_read_tr16_b64_v4i16((lds_s16x4_p)(p + hi_off));
  u32x2 a0 = __builtin_bit_cast(u32x2, v0), a1 = __builtin_bit_cast(u32x2, v1);
  return u32x4{a0[0], a0[1], a1[0], a1[1]};
}

__device__ __forceinline__ void dma4(unsigned m0v, __amdgpu_buffer_rsrc_t r, unsigned soff,
                                     unsigned v0, unsigned v1, unsigned v2, unsigned v3) {
  asm volatile(
      "s_mov_b32 m0, %0\n\ts_nop 0\n\t"
      "buffer_load_dwordx4 %1, %5, %6 offen nt lds\n\t"
      "buffer_load_dwordx4 %2, %5, %6 offen offset:1024 nt lds\n\t"
      "buffer_load_dwordx4 %3, %5, %6 offen offset:2048 nt lds\n\t"
      "buffer_load_dwordx4 %4, %5, %6 offen offset:3072 nt lds"
      :: "s"(m0v), "v"(v0), "v"(v1), "v"(v2), "v"(v3), "s"(r), "s"(soff) : "memory");
}
__device__ __forceinline__ void dma2(unsigned m0v, __amdgpu_buffer_rsrc_t r, unsigned soff, unsigned v0, unsigned v1) {
  asm volatile(
      "s_mov_b32 m0, %0\n\ts_nop 0\n\t"
      "buffer_load_dwordx4 %1, %3, %4 offen lds\n\t"
      "buffer_load_dwordx4 %2, %3, %4 offen offset:1024 lds"
      :: "s"(m0v), "v"(v0), "v"(v1), "s"(r), "s"(soff) : "memory");
}
__device__ __forceinline__ void dma2w(unsigned m0v, __amdgpu_buffer_rsrc_t r, unsigned soff, unsigned v0, unsigned v1) {
  asm volatile(
      "s_mov_b32 m0, %0\n\ts_nop 0\n\t"
      "buffer_load_dwordx4 %1, %3, %4 offen nt lds\n\t"
      "buffer_load_dwordx4 %2, %3, %4 offen offset:1024 nt lds"
      :: "s"(m0v), "v"(v0), "v"(v1), "s"(r), "s"(soff) : "memory");
}
template <int N> __device__ __forceinline__ void wait_vm() {
  asm volatile("s_waitcnt vmcnt(%0) lgkmcnt(0)" :: "n"(N) : "memory");
}
template <int V> struct IC { static constexpr int value = V; };
template <class F> __device__ __forceinline__ void static_for16(F&& f) {
  f(IC<0>{}); f(IC<1>{}); f(IC<2>{}); f(IC<3>{}); f(IC<4>{}); f(IC<5>{}); f(IC<6>{}); f(IC<7>{});
  f(IC<8>{}); f(IC<9>{}); f(IC<10>{}); f(IC<11>{}); f(IC<12>{}); f(IC<13>{}); f(IC<14>{}); f(IC<15>{});
}
constexpr int qslot(int kk) { return kk % QNW; }
constexpr int qwait(int kk, int nx) {
  if (kk >= 15) return 63;
  int n = 0, wend[16] = {}, xend[8] = {};
  for (int i = 0; i < QNW; i++) {
    if (i != 1) { n += nx; xend[(i + 1) / 2] = n; }
    n += 6; wend[i] = n;
  }
  for (int k = 0; k < kk; k++) {
    if (k + QNW < 16) { n += 6; wend[k + QNW] = n; }
    if ((k & 1) && k < 11) { n += nx; xend[(k + 5) / 2] = n; }
    if (k == KV0_STEP) n += 12;
  }
  const int xe = nx ? xend[(kk + 1) / 2] : 0;
  const int need = wend[kk + 1] > xe ? wend[kk + 1] : xe;
  return n - need;
}
__device__ __forceinline__ void scale_acc(f32x4& x, float alpha) {
  asm volatile("" : "+a"(x));
  x *= alpha;
  asm volatile("" : "+a"(x));
}

__global__ __launch_bounds__(256, 1) void attn_kernel(
    const char* __restrict__ xA, const char* __restrict__ KA, const char* __restrict__ VA, const char* __restrict__ wqA,
    const char* __restrict__ xB, const char* __restrict__ KB, const char* __restrict__ VB, const char* __restrict__ wqB,
    const char* __restrict__ woA, const char* __restrict__ woB, u32x4* __restrict__ Obuf) {
  extern __shared__ __attribute__((aligned(16))) char smem[];
  const int bid = blockIdx.x, xj = bid & 7, ci = bid >> 3;
  const int w = __builtin_amdgcn_readfirstlane(threadIdx.x >> 6), l = threadIdx.x & 63;
  const int r = l & 15, g = l >> 4;
  const unsigned ldsbase = (unsigned)(uintptr_t)(lds_char_p)smem;

  const int hM = 4 * xj + (ci & 3), bM = 2 * (ci >> 2);
  const int bG = ci >> 1, kvh = xj;
  const int hG = 4 * kvh + 2 * (ci & 1);
  const size_t U = (size_t)SK * DH * 2;

  __amdgpu_buffer_rsrc_t drs[3];
  unsigned dvo[3][4], dso[3], dm0[3];
#pragma unroll
  for (int e = 0; e < 3; e++) {
    const int G = 3 * w + e, st = G >> 2, isV = (G >> 1) & 1, hf = G & 1;
    const char* base = st == 2 ? (isV ? VA : KA) + (size_t)(bG * 8 + kvh) * U
                               : (isV ? VB : KB) + (size_t)((bM + st) * NH + hM) * U;
    drs[e] = mkrsrc(rfl_ptr(base), (unsigned)U);
    dso[e] = hf * 4096;
    dm0[e] = ldsbase + G * 4096;
#pragma unroll
    for (int i = 0; i < 4; i++) {
      const int sw = isV ? ((4 * (i & 1) + g) << 1) : (4 * g + i);
      dvo[e][i] = g * 256 + ((r ^ sw) << 4);
    }
  }
  auto issue_group = [&](int e, int t, int slot) {
    dma4(dm0[e] + slot * STAGE, drs[e], dso[e] + t * 8192, dvo[e][0], dvo[e][1], dvo[e][2], dvo[e][3]);
  };
  const int tb = w < 2 ? bM + w : bG, th = w < 2 ? hM : hG + (w - 2);
  u32x4 qf[3][4];
  {
    __amdgpu_buffer_rsrc_t rwM = mkrsrc(rfl_ptr(wqB + (size_t)hM * DH * 2), (DM - 1) * NH * DH * 2 + DH * 2);
    __amdgpu_buffer_rsrc_t rwG = mkrsrc(rfl_ptr(wqA + (size_t)hG * DH * 2), (DM - 1) * NH * DH * 2 + 2 * DH * 2);
    const unsigned wv0 = (8 * w + g) * (NH * DH * 2) + ((r ^ (g | ((w & 1) << 3))) << 4);
    const unsigned wv1 = wv0 + 4 * (NH * DH * 2) - 1024;
    __amdgpu_buffer_rsrc_t rx = mkrsrc(rfl_ptr((w < 2 ? xB : xA) + (size_t)tb * SQ * DM * 2), SQ * DM * 2);
    const unsigned xv0 = (l >> 3) * (DM * 2) + (((l & 7) ^ g) << 4);
    const unsigned xv1 = (l >> 3) * (DM * 2) + (((l & 7) ^ (4 + g)) << 4) + 8 * (DM * 2) - 1024;
    const int rot = 2 * (ci >> 2);
    auto ldw = [&](int kk, int i) {
      const unsigned so = ((kk + rot) & 15) * 32 * (NH * DH * 2), m = ldsbase + QW_OFF + qslot(kk) * QW_SLOT + w * 2048;
      dma2w(m + i * 8192, i == 0 ? rwM : rwG, so + (i == 2 ? DH * 2 : 0), wv0, wv1);
    };
    auto ldx = [&](int j, int i) {
      const unsigned so = ((2 * j + rot) & 15) * 64, m = ldsbase + QX_OFF + (j % QNX) * QX_SLOT + w * 6144;
      if (w < 3) dma2(m + i * 2048, rx, so + i * 16 * (DM * 2), xv0, xv1);
    };
#pragma unroll
    for (int k = 0; k < QNW; k++) {
#pragma unroll
      for (int i = 0; i < 3; i++) if (k != 1) ldx((k + 1) / 2, i);
#pragma unroll
      for (int i = 0; i < 3; i++) ldw(k, i);
    }
    f32x4 qa[8][3];
#pragma unroll
    for (int i = 0; i < 8; i++)
#pragma unroll
      for (int qb = 0; qb < 3; qb++) qa[i][qb] = f32x4{0.f, 0.f, 0.f, 0.f};
    const unsigned abase = QW_OFF + (w < 2 ? 0 : w - 1) * 8192 + (8 * g + (r >> 2)) * 256 + ((r & 2) << 2);
    const unsigned asw = (r >> 2) | ((g & 1) << 3), ach = 4 * (r & 3);
    const unsigned xbase = QX_OFF + (w < 2 ? w : 2) * 6144 + r * 128 + ((g ^ (r >> 1)) << 4);
    u32x4 af[2][8], xf[2][3];
    auto rdx = [&](int kk, int qb) {
      xf[kk & 1][qb] = *(const u32x4*)(smem + ((kk >> 1) % QNX) * QX_SLOT + ((xbase + qb * 2048) ^ ((kk & 1) << 6)));
    };
    auto rda = [&](int kk, int i) {
      af[kk & 1][i] = tr_frag(smem + qslot(kk) * QW_SLOT + ((abase + (((ach + (i >> 1)) ^ asw) << 4)) ^ ((i & 1) << 3)), 1024);
    };
    if (w < 3) wait_vm<qwait(-1, 6)>(); else wait_vm<qwait(-1, 0)>();
    asm volatile("s_barrier" ::: "memory");
#pragma unroll
    for (int qb = 0; qb < 3; qb++) rdx(0, qb);
#pragma unroll
    for (int i = 0; i < 8; i++) rda(0, i);
    static_for16([&](auto kc) {
      constexpr int kk = decltype(kc)::value;
      if (w < 3) wait_vm<qwait(kk, 6)>(); else wait_vm<qwait(kk, 0)>();
      asm volatile("s_barrier" ::: "memory");
#pragma unroll
      for (int i = 0; i < 8; i++) {
        if (kk < 15) {
          if (i < 3) rdx(kk + 1, i);
          rda(kk + 1, i);
        }
#pragma unroll
        for (int qb = 0; qb < 3; qb++) qa[i][qb] = mfma16(af[kk & 1][i], xf[kk & 1][qb], qa[i][qb]);
        if (kk + QNW < 16 && i < 3) ldw(kk + QNW, i);
        if ((kk & 1) && kk < 11 && i >= 3 && i < 6) ldx((kk + 5) / 2, i - 3);
        if (kk == KV0_STEP && i < 3) issue_group(i, 0, 0);
        if (kk == 15 && i >= 5) issue_group(i - 5, 1, 1);
        __builtin_amdgcn_sched_barrier(0);
      }
    });
    const bool odd = g >> 1;
#pragma unroll
    for (int qb = 0; qb < 3; qb++)
#pragma unroll
      for (int s = 0; s < 4; s++) {
        const unsigned a0 = pack2(qa[2 * s][qb][0], qa[2 * s][qb][1]), a1 = pack2(qa[2 * s][qb][2], qa[2 * s][qb][3]);
        const unsigned b0 = pack2(qa[2 * s + 1][qb][0], qa[2 * s + 1][qb][1]), b1 = pack2(qa[2 * s + 1][qb][2], qa[2 * s + 1][qb][3]);
        qf[qb][s] = u32x4{odd ? b0 : a0, odd ? b1 : a1, odd ? a0 : b0, odd ? a1 : b1};
      }
  }
#pragma unroll
  for (int e = 0; e < 3; e++) issue_group(e, 2, 2);

  const int strm = w < 2 ? w : 2;
  unsigned kaddr[4], vaddr0;
#pragma unroll
  for (int s = 0; s < 4; s++) kaddr[s] = strm * 16384 + r * 256 + (((4 * g + s) ^ (4 * (r & 3) + (r >> 2))) << 4);
  {
    const int row = 4 * g + (r >> 2), p = r & 3;
    vaddr0 = strm * 16384 + 8192 + row * 256 + ((((p >> 1)) ^ ((row & 7) << 1)) << 4) + (p & 1) * 8;
  }
  f32x4 oa[8][3];
#pragma unroll
  for (int i = 0; i < 8; i++)
#pragma unroll
    for (int qb = 0; qb < 3; qb++) oa[i][qb] = f32x4{0.f, 0.f, 0.f, 0.f};
  float mrow[3] = {-INFINITY, -INFINITY, -INFINITY}, lsum[3] = {0.f, 0.f, 0.f};

  asm volatile("s_waitcnt vmcnt(24) lgkmcnt(0)\n\ts_barrier" ::: "memory");
  int slot = 0;
  const int wu = ((ci * 4 + w) * 2 + ((xj >> 1) & 1));
  for (int t = 0; t < NT; t++) {
    const bool refill = t + 3 < NT;
    const char* sb = smem + slot * STAGE;
    u32x4 kf[2][4], vf[8];
#pragma unroll
    for (int s = 0; s < 4; s++)
#pragma unroll
      for (int kb = 0; kb < 2; kb++) kf[kb][s] = *(const u32x4*)(sb + kaddr[s] + kb * 4096);
    __builtin_amdgcn_sched_barrier(0);
    f32x4 sa[2][3];
#pragma unroll
    for (int s = 0; s < 4; s++) {
#pragma unroll
      for (int kb = 0; kb < 2; kb++)
#pragma unroll
        for (int qb = 0; qb < 3; qb++)
          sa[kb][qb] = mfma16(kf[kb][s], qf[qb][s], s == 0 ? f32x4{0.f, 0.f, 0.f, 0.f} : sa[kb][qb]);
      vf[2 * s] = tr_frag(sb + (vaddr0 ^ ((2 * s) << 5)), 4096);
      vf[2 * s + 1] = tr_frag(sb + (vaddr0 ^ ((2 * s + 1) << 5)), 4096);
      __builtin_amdgcn_sched_barrier(0);
    }
    asm volatile("s_waitcnt lgkmcnt(0)\n\ts_barrier" ::: "memory");
    if (refill) { issue_group(0, t + 3, slot); issue_group(1, t + 3, slot); }
    if (t == NT - 3) {
      __amdgpu_buffer_rsrc_t rp = mkrsrc(rfl_ptr((xj & 4 ? woB : woA) + (size_t)wu * 16384 + (xj & 1) * 512 - 3072), 18944);
      const unsigned pv = 3072 + (l >> 3) * 1024 + (l & 7) * 16;
      dma4(ldsbase + 2 * STAGE + w * 8192, rp, 0, pv, pv - 896, pv - 1792, pv - 2688);
      dma4(ldsbase + 2 * STAGE + w * 8192 + 4096, rp, 8192, pv, pv - 896, pv - 1792, pv - 2688);
    }
    float mx[3];
    bool need = false;
#pragma unroll
    for (int qb = 0; qb < 3; qb++) {
      float m0 = __builtin_fmaxf(__builtin_fmaxf(sa[0][qb][0], sa[0][qb][1]), sa[0][qb][2]);
      m0 = __builtin_fmaxf(__builtin_fmaxf(m0, sa[0][qb][3]), sa[1][qb][0]);
      m0 = __builtin_fmaxf(__builtin_fmaxf(m0, sa[1][qb][1]), sa[1][qb][2]);
      mx[qb] = __builtin_fmaxf(m0, sa[1][qb][3]);
      need |= mx[qb] > mrow[qb] + RESCALE_THR;
    }
    if (__builtin_expect(__builtin_amdgcn_ballot_w64(need) != 0, 0)) {
#pragma unroll
      for (int qb = 0; qb < 3; qb++) {
        float m = mx[qb];
        m = fmaxf(m, __shfl_xor(m, 16));
        m = fmaxf(m, __shfl_xor(m, 32));
        m = fmaxf(m, mrow[qb]);
        const float alpha = __builtin_amdgcn_exp2f((mrow[qb] - m) * SCL2);
        mrow[qb] = m;
        lsum[qb] *= alpha;
#pragma unroll
        for (int i = 0; i < 8; i++) scale_acc(oa[i][qb], alpha);
      }
    }
    u32x4 pf[3];
#pragma unroll
    for (int qb = 0; qb < 3; qb++) {
      const float mc = mrow[qb] * SCL2;
      float p[8];
#pragma unroll
      for (int kb = 0; kb < 2; kb++)
#pragma unroll
        for (int e = 0; e < 4; e++) p[kb * 4 + e] = __builtin_amdgcn_exp2f(__builtin_fmaf(sa[kb][qb][e], SCL2, -mc));
      lsum[qb] += ((p[0] + p[1]) + (p[2] + p[3])) + ((p[4] + p[5]) + (p[6] + p[7]));
#pragma unroll
      for (int e = 0; e < 4; e++) pf[qb][e] = pack2(p[2 * e], p[2 * e + 1]);
    }
    if (refill) issue_group(2, t + 3, slot);
#pragma unroll
    for (int qb = 0; qb < 3; qb++) {
#pragma unroll
      for (int db = 0; db < 8; db++) oa[db][qb] = mfma16(vf[db], pf[qb], oa[db][qb]);
    }
    slot = slot == 2 ? 0 : slot + 1;
    if (t >= NT - 2) asm volatile("s_waitcnt vmcnt(0) lgkmcnt(0)\n\ts_barrier" ::: "memory");
    else if (t == NT - 3) asm volatile("s_waitcnt vmcnt(20) lgkmcnt(0)\n\ts_barrier" ::: "memory");
    else asm volatile("s_waitcnt vmcnt(24) lgkmcnt(0)\n\ts_barrier" ::: "memory");
  }

  __amdgpu_buffer_rsrc_t rsw = mkrsrc(rfl_ptr((const char*)(Obuf + WPK_OFF + ((xj >> 2) * 8 + (xj & 1) * 4) * 32768 + wu * 128)),
                                      3 * 524288 + 2048);
#pragma unroll
  for (int i = 0; i < 8; i++)
    __builtin_amdgcn_raw_buffer_store_b128(*(const u32x4*)(smem + 2 * STAGE + w * 8192 + i * 1024 + l * 16), rsw, l * 16,
                                           (i & 3) * 524288 + (i >> 2) * 1024, WT);
  const int task = ((w < 2 ? 1 : 0) * NB + tb) * NH + th;
  __amdgpu_buffer_rsrc_t rso = mkrsrc(rfl_ptr((const char*)(Obuf + (size_t)task * 12 * 64)), 12 * 1024);
#pragma unroll
  for (int qb = 0; qb < 3; qb++) {
    float s = lsum[qb];
    s += __shfl_xor(s, 16);
    s += __shfl_xor(s, 32);
    const float inv = 1.0f / s;
#pragma unroll
    for (int u = 0; u < 4; u++) {
      u32x4 o;
      o[0] = pack2(oa[2 * u][qb][0] * inv, oa[2 * u][qb][1] * inv);
      o[1] = pack2(oa[2 * u][qb][2] * inv, oa[2 * u][qb][3] * inv);
      o[2] = pack2(oa[2 * u + 1][qb][0] * inv, oa[2 * u + 1][qb][1] * inv);
      o[3] = pack2(oa[2 * u + 1][qb][2] * inv, oa[2 * u + 1][qb][3] * inv);
      __builtin_amdgcn_raw_buffer_store_b128(o, rso, l * 16, (u * 3 + qb) * 1024, WT);
    }
  }
}

constexpr int OQD = 6;
constexpr int OP_LDS = 4 * OQD * 4096;
__global__ __launch_bounds__(256, 1) void oproj_kernel(const u32x4* __restrict__ Obuf, char* __restrict__ out) {
  extern __shared__ __attribute__((aligned(16))) char smem[];
  const int bid = blockIdx.x, xj = bid & 7, ci = bid >> 3;
  const int br = xj >> 2, b = 8 * ((xj >> 1) & 1) + (ci >> 2), ns = (4 * (xj & 1) + (ci & 3));
  const int w = __builtin_amdgcn_readfirstlane(threadIdx.x >> 6), l = threadIdx.x & 63;
  const int r = l & 15, g = l >> 4;
  const unsigned ring = (unsigned)(uintptr_t)(lds_char_p)smem + w * (OQD * 4096);
  const char* wp = rfl_ptr((const char*)(Obuf + WPK_OFF + (br * 8 + ns) * 32768 + w * 8192));
  __amdgpu_buffer_rsrc_t rw = mkrsrc(wp, 1024 * 128);
  const char* op = rfl_ptr((const char*)(Obuf + (size_t)((br * NB + b) * NH + w * 8) * 12 * 64));
  __amdgpu_buffer_rsrc_t ro = mkrsrc(op, 8 * 12 * 1024);
  const unsigned wvo = (l >> 3) * 128 + (((l & 7) ^ (((l >> 4) & 3) << 1)) << 4);
  const unsigned ovo = l * 16;
  u32x4 ob[OQD][3];
  f32x4 acc[4][3];
#pragma unroll
  for (int i = 0; i < 4; i++)
#pragma unroll
    for (int qb = 0; qb < 3; qb++) acc[i][qb] = f32x4{0.f, 0.f, 0.f, 0.f};
  auto ldw = [&](int kk, int sl) {
    asm volatile(
        "s_mov_b32 m0, %0\n\ts_nop 0\n\t"
        "buffer_load_dwordx4 %1, %5, %6 offen lds\n\t"
        "buffer_load_dwordx4 %2, %5, %6 offen offset:1024 lds\n\t"
        "buffer_load_dwordx4 %3, %5, %6 offen offset:2048 lds\n\t"
        "buffer_load_dwordx4 %4, %5, %6 offen offset:3072 lds"
        :: "s"(ring + sl * 4096), "v"(wvo), "v"(wvo), "v"(wvo), "v"(wvo), "s"(rw),
           "s"(kk * 4096)
        : "memory");
  };
  auto ldo = [&](int kk, int sl) {
    asm volatile(
        "buffer_load_dwordx4 %0, %3, %4, %5 offen\n\t"
        "buffer_load_dwordx4 %1, %3, %4, %5 offen offset:1024\n\t"
        "buffer_load_dwordx4 %2, %3, %4, %5 offen offset:2048"
        : "=&v"(ob[sl][0]), "=&v"(ob[sl][1]), "=&v"(ob[sl][2])
        : "v"(ovo), "s"(ro), "s"(kk * 3072)
        : "memory");
  };
  const int row = 4 * g + (r >> 2), p = r & 3;
  const unsigned aoff = w * (OQD * 4096) + row * 128 + (((p >> 1) ^ (((row >> 1) & 3) << 1)) << 4) + (p & 1) * 8;
#pragma unroll
  for (int kk = 0; kk < OQD; kk++) { ldw(kk, kk); ldo(kk, kk); }
  u32x4 af[2][4];
  asm volatile("s_waitcnt vmcnt(%0)" :: "n"(7 * OQD - 4) : "memory");
#pragma unroll
  for (int db = 0; db < 4; db++) af[0][db] = tr_frag(smem + (aoff ^ (db << 5)), 2048);
  auto step = [&](auto kc) {
    constexpr int kk = decltype(kc)::value, sl = kk % OQD;
    constexpr int last = kk + OQD - 1 < 31 ? kk + OQD - 1 : 31;
    constexpr int allow = kk < 31 ? 7 * (last - kk - 1) + 3 : 0;
    asm volatile("s_waitcnt vmcnt(%3) lgkmcnt(0)" : "+v"(ob[sl][0]), "+v"(ob[sl][1]), "+v"(ob[sl][2]) : "n"(allow) : "memory");
    if (kk < 31) {
#pragma unroll
      for (int db = 0; db < 4; db++) af[(kk + 1) & 1][db] = tr_frag(smem + ((kk + 1) % OQD) * 4096 + (aoff ^ (db << 5)), 2048);
    }
#pragma unroll
    for (int db = 0; db < 2; db++)
#pragma unroll
      for (int qb = 0; qb < 3; qb++) acc[db][qb] = mfma16(af[kk & 1][db], ob[sl][qb], acc[db][qb]);
    __builtin_amdgcn_sched_barrier(0);
    if (kk + OQD < 32) ldw(kk + OQD, sl);
#pragma unroll
    for (int db = 2; db < 4; db++)
#pragma unroll
      for (int qb = 0; qb < 3; qb++) acc[db][qb] = mfma16(af[kk & 1][db], ob[sl][qb], acc[db][qb]);
    __builtin_amdgcn_sched_barrier(0);
    if (kk + OQD < 32) ldo(kk + OQD, sl);
  };
  static_for16(step);
  static_for16([&](auto kc) { step(IC<16 + decltype(kc)::value>{}); });
  f32x4* red = (f32x4*)smem;
#pragma unroll
  for (int qb = 0; qb < 3; qb++)
#pragma unroll
    for (int db = 0; db < 4; db++) red[w * (OQD * 256) + (qb * 4 + db) * 64 + l] = acc[db][qb];
  __syncthreads();
  __amdgpu_buffer_rsrc_t rout = mkrsrc(rfl_ptr(out + (size_t)b * SQ * 2048 + br * 1024 + ns * 128), (SQ - 1) * 2048 + 128);
#pragma unroll
  for (int it = 0; it < 2; it++) {
    const int wv = w + 4 * it;
    if (wv < 6) {
      const int q = 8 * wv + (l >> 3), c8 = l & 7;
      const int qb = q >> 4, idx = (qb * 4 + (c8 >> 1)) * 64 + (2 * (c8 & 1)) * 16 + (q & 15);
      f32x4 s0 = {0.f, 0.f, 0.f, 0.f}, s1 = s0;
#pragma unroll
      for (int ww = 0; ww < 4; ww++) {
        s0 += red[ww * (OQD * 256) + idx];
        s1 += red[ww * (OQD * 256) + idx + 16];
      }
      u32x4 o = {pack2(s0[0], s0[1]), pack2(s0[2], s0[3]), pack2(s1[0], s1[1]), pack2(s1[2], s1[3])};
      __builtin_amdgcn_raw_buffer_store_b128(o, rout, q * 2048 + c8 * 16, 0, WT);
    }
  }
}

extern "C" void kernel_launch(void* const* d_in, const int* in_sizes, int n_in, void* d_out, int out_size,
                              void* d_ws, size_t ws_size, hipStream_t stream) {
  const char* xA = (const char*)d_in[0]; const char* KA = (const char*)d_in[1]; const char* VA = (const char*)d_in[2];
  const char* wqA = (const char*)d_in[3]; const char* woA = (const char*)d_in[4];
  const char* xB = (const char*)d_in[5]; const char* KB = (const char*)d_in[6]; const char* VB = (const char*)d_in[7];
  const char* wqB = (const char*)d_in[8]; const char* woB = (const char*)d_in[9];
  u32x4* Obuf = (u32x4*)d_ws;
  (void)hipFuncSetAttribute((const void*)attn_kernel, hipFuncAttributeMaxDynamicSharedMemorySize, ATTN_LDS);
  (void)hipFuncSetAttribute((const void*)oproj_kernel, hipFuncAttributeMaxDynamicSharedMemorySize, OP_LDS);
  attn_kernel<<<256, 256, ATTN_LDS, stream>>>(xA, KA, VA, wqA, xB, KB, VB, wqB, woA, woB, Obuf);
  oproj_kernel<<<256, 256, OP_LDS, stream>>>(Obuf, (char*)d_out);
}
```

```cpp
#include <hip/hip_runtime.h>
#include <hip/hip_bf16.h>
#include <stdint.h>

typedef short s16x4 __attribute__((ext_vector_type(4)));
typedef float f32x4 __attribute__((ext_vector_type(4)));
typedef unsigned u32x4 __attribute__((ext_vector_type(4)));
typedef unsigned u32x2 __attribute__((ext_vector_type(2)));
typedef __bf16 bf16x8 __attribute__((ext_vector_type(8)));
typedef __bf16 bf16x2 __attribute__((ext_vector_type(2)));
typedef float f32x2 __attribute__((ext_vector_type(2)));
typedef __attribute__((address_space(3))) s16x4* lds_s16x4_p;
typedef __attribute__((address_space(3))) char* lds_char_p;

constexpr int NB = 16, SQ = 48, DM = 512, NH = 32, SK = 1024, DH = 128;
constexpr int STAGE = 49152;
constexpr int NSLOT = 3;
constexpr int NT = SK / 32;
constexpr int ATTN_LDS = NSLOT * STAGE + 16384;
constexpr float SCL2 = 0.08838834764831845f * 1.4426950408889634f;
constexpr float RESCALE_THR = 8.0f / SCL2;
constexpr int QW_SLOT = 24576, QX_SLOT = 18432;
constexpr int KV0_STEP = 13;
constexpr int WT = 16;
constexpr int QNW = 4, QNX = 3;
constexpr int WPK_OFF = 1 << 20;
constexpr int QW_OFF = ATTN_LDS - QNW * QW_SLOT - QNX * QX_SLOT, QX_OFF = ATTN_LDS - QNX * QX_SLOT;


__device__ __forceinline__ unsigned pack2(float a, float b) {
  f32x2 v = {a, b};
  bf16x2 r = __builtin_convertvector(v, bf16x2);
  return __builtin_bit_cast(unsigned, r);
}
__device__ __forceinline__ f32x4 mfma16(u32x4 a, u32x4 b, f32x4 c) {
  return __builtin_amdgcn_mfma_f32_16x16x32_bf16(__builtin_bit_cast(bf16x8, a), __builtin_bit_cast(bf16x8, b), c, 0, 0, 0);
}
__device__ __forceinline__ __amdgpu_buffer_rsrc_t mkrsrc(const void* p, unsigned bytes) {
  return __builtin_amdgcn_make_buffer_rsrc((void*)p, 0, bytes, 0x00020000);
}
__device__ __forceinline__ const char* rfl_ptr(const char* p) {
  uint64_t v = (uint64_t)p;
  unsigned lo = __builtin_amdgcn_readfirstlane((unsigned)v), hi = __builtin_amdgcn_readfirstlane((unsigned)(v >> 32));
  return (const char*)(((uint64_t)hi << 32) | lo);
}
__device__ __forceinline__ u32x4 tr_frag(const char* p, int hi_off) {
  s16x4 v0 = __builtin_amdgcn_ds_read_tr16_b64_v4i16((lds_s16x4_p)(p));
  s16x4 v1 = __builtin_amdgcn_ds_read_tr16_b64_v4i16((lds_s16x4_p)(p + hi_off));
  u32x2 a0 = __builtin_bit_cast(u32x2, v0), a1 = __builtin_bit_cast(u32x2, v1);
  return u32x4{a0[0], a0[1], a1[0], a1[1]};
}

__device__ __forceinline__ void dma4(unsigned m0v, __amdgpu_buffer_rsrc_t r, unsigned soff,
                                     unsigned v0, unsigned v1, unsigned v2, unsigned v3) {
  asm volatile(
      "s_mov_b32 m0, %0\n\ts_nop 0\n\t"
      "buffer_load_dwordx4 %1, %5, %6 offen nt lds\n\t"
      "buffer_load_dwordx4 %2, %5, %6 offen offset:1024 nt lds\n\t"
      "buffer_load_dwordx4 %3, %5, %6 offen offset:2048 nt lds\n\t"
      "buffer_load_dwordx4 %4, %5, %6 offen offset:3072 nt lds"
      :: "s"(m0v), "v"(v0), "v"(v1), "v"(v2), "v"(v3), "s"(r), "s"(soff) : "memory");
}
__device__ __forceinline__ void dma2(unsigned m0v, __amdgpu_buffer_rsrc_t r, unsigned soff, unsigned v0, unsigned v1) {
  asm volatile(
      "s_mov_b32 m0, %0\n\ts_nop 0\n\t"
      "buffer_load_dwordx4 %1, %3, %4 offen lds\n\t"
      "buffer_load_dwordx4 %2, %3, %4 offen offset:1024 lds"
      :: "s"(m0v), "v"(v0), "v"(v1), "s"(r), "s"(soff) : "memory");
}
template <int N> __device__ __forceinline__ void wait_vm() {
  asm volatile("s_waitcnt vmcnt(%0) lgkmcnt(0)" :: "n"(N) : "memory");
}
template <int V> struct IC { static constexpr int value = V; };
template <class F> __device__ __forceinline__ void static_for16(F&& f) {
  f(IC<0>{}); f(IC<1>{}); f(IC<2>{}); f(IC<3>{}); f(IC<4>{}); f(IC<5>{}); f(IC<6>{}); f(IC<7>{});
  f(IC<8>{}); f(IC<9>{}); f(IC<10>{}); f(IC<11>{}); f(IC<12>{}); f(IC<13>{}); f(IC<14>{}); f(IC<15>{});
}
constexpr int qslot(int kk) { return kk % QNW; }
constexpr int qwait(int kk, int nx) {
  if (kk >= 15) return 63;
  int n = 0, wend[16] = {}, xend[8] = {};
  for (int i = 0; i < QNW; i++) {
    if (i != 1) { n += nx; xend[(i + 1) / 2] = n; }
    n += 6; wend[i] = n;
  }
  for (int k = 0; k < kk; k++) {
    if (k + QNW < 16) { n += 6; wend[k + QNW] = n; }
    if ((k & 1) && k < 11) { n += nx; xend[(k + 5) / 2] = n; }
    if (k == KV0_STEP) n += 12;
  }
  const int xe = nx ? xend[(kk + 1) / 2] : 0;
  const int need = wend[kk + 1] > xe ? wend[kk + 1] : xe;
  return n - need;
}
__device__ __forceinline__ void scale_acc(f32x4& x, float alpha) {
  asm volatile("" : "+a"(x));
  x *= alpha;
  asm volatile("" : "+a"(x));
}

__global__ __launch_bounds__(256, 1) void attn_kernel(
    const char* __restrict__ xA, const char* __restrict__ KA, const char* __restrict__ VA, const char* __restrict__ wqA,
    const char* __restrict__ xB, const char* __restrict__ KB, const char* __restrict__ VB, const char* __restrict__ wqB,
    const char* __restrict__ woA, const char* __restrict__ woB, u32x4* __restrict__ Obuf) {
  extern __shared__ __attribute__((aligned(16))) char smem[];
  const int bid = blockIdx.x, xj = bid & 7, ci = bid >> 3;
  const int w = __builtin_amdgcn_readfirstlane(threadIdx.x >> 6), l = threadIdx.x & 63;
  const int r = l & 15, g = l >> 4;
  const unsigned ldsbase = (unsigned)(uintptr_t)(lds_char_p)smem;

  const int hM = 4 * xj + (ci & 3), bM = 2 * (ci >> 2);
  const int bG = ci >> 1, kvh = xj;
  const int hG = 4 * kvh + 2 * (ci & 1);
  const size_t U = (size_t)SK * DH * 2;

  __amdgpu_buffer_rsrc_t drs[3];
  unsigned dvo[3][4], dso[3], dm0[3];
#pragma unroll
  for (int e = 0; e < 3; e++) {
    const int G = 3 * w + e, st = G >> 2, isV = (G >> 1) & 1, hf = G & 1;
    const char* base = st == 2 ? (isV ? VA : KA) + (size_t)(bG * 8 + kvh) * U
                               : (isV ? VB : KB) + (size_t)((bM + st) * NH + hM) * U;
    drs[e] = mkrsrc(rfl_ptr(base), (unsigned)U);
    dso[e] = hf * 4096;
    dm0[e] = ldsbase + G * 4096;
#pragma unroll
    for (int i = 0; i < 4; i++) {
      const int sw = isV ? ((4 * (i & 1) + g) << 1) : (4 * g + i);
      dvo[e][i] = g * 256 + ((r ^ sw) << 4);
    }
  }
  const int trot = 5 * (ci >> 1) + 11 * xj;
  auto issue_group = [&](int e, int t, int slot) {
    dma4(dm0[e] + slot * STAGE, drs[e], dso[e] + ((t + trot) & (NT - 1)) * 8192, dvo[e][0], dvo[e][1], dvo[e][2], dvo[e][3]);
  };
  const int tb = w < 2 ? bM + w : bG, th = w < 2 ? hM : hG + (w - 2);
  u32x4 qf[3][4];
  {
    __amdgpu_buffer_rsrc_t rwM = mkrsrc(rfl_ptr(wqB + (size_t)hM * DH * 2), (DM - 1) * NH * DH * 2 + DH * 2);
    __amdgpu_buffer_rsrc_t rwG = mkrsrc(rfl_ptr(wqA + (size_t)hG * DH * 2), (DM - 1) * NH * DH * 2 + 2 * DH * 2);
    const unsigned wv0 = (8 * w + g) * (NH * DH * 2) + ((r ^ (g | ((w & 1) << 3))) << 4);
    const unsigned wv1 = wv0 + 4 * (NH * DH * 2) - 1024;
    __amdgpu_buffer_rsrc_t rx = mkrsrc(rfl_ptr((w < 2 ? xB : xA) + (size_t)tb * SQ * DM * 2), SQ * DM * 2);
    const unsigned xv0 = (l >> 3) * (DM * 2) + (((l & 7) ^ g) << 4);
    const unsigned xv1 = (l >> 3) * (DM * 2) + (((l & 7) ^ (4 + g)) << 4) + 8 * (DM * 2) - 1024;
    const int rot = 2 * (ci >> 2);
    auto ldw = [&](int kk, int i) {
      const unsigned so = ((kk + rot) & 15) * 32 * (NH * DH * 2), m = ldsbase + QW_OFF + qslot(kk) * QW_SLOT + w * 2048;
      dma2(m + i * 8192, i == 0 ? rwM : rwG, so + (i == 2 ? DH * 2 : 0), wv0, wv1);
    };
    auto ldx = [&](int j, int i) {
      const unsigned so = ((2 * j + rot) & 15) * 64, m = ldsbase + QX_OFF + (j % QNX) * QX_SLOT + w * 6144;
      if (w < 3) dma2(m + i * 2048, rx, so + i * 16 * (DM * 2), xv0, xv1);
    };
#pragma unroll
    for (int k = 0; k < QNW; k++) {
#pragma unroll
      for (int i = 0; i < 3; i++) if (k != 1) ldx((k + 1) / 2, i);
#pragma unroll
      for (int i = 0; i < 3; i++) ldw(k, i);
    }
    f32x4 qa[8][3];
#pragma unroll
    for (int i = 0; i < 8; i++)
#pragma unroll
      for (int qb = 0; qb < 3; qb++) qa[i][qb] = f32x4{0.f, 0.f, 0.f, 0.f};
    const unsigned abase = QW_OFF + (w < 2 ? 0 : w - 1) * 8192 + (8 * g + (r >> 2)) * 256 + ((r & 2) << 2);
    const unsigned asw = (r >> 2) | ((g & 1) << 3), ach = 4 * (r & 3);
    const unsigned xbase = QX_OFF + (w < 2 ? w : 2) * 6144 + r * 128 + ((g ^ (r >> 1)) << 4);
    u32x4 af[2][8], xf[2][3];
    auto rdx = [&](int kk, int qb) {
      xf[kk & 1][qb] = *(const u32x4*)(smem + ((kk >> 1) % QNX) * QX_SLOT + ((xbase + qb * 2048) ^ ((kk & 1) << 6)));
    };
    auto rda = [&](int kk, int i) {
      af[kk & 1][i] = tr_frag(smem + qslot(kk) * QW_SLOT + ((abase + (((ach + (i >> 1)) ^ asw) << 4)) ^ ((i & 1) << 3)), 1024);
    };
    if (w < 3) wait_vm<qwait(-1, 6)>(); else wait_vm<qwait(-1, 0)>();
    asm volatile("s_barrier" ::: "memory");
#pragma unroll
    for (int qb = 0; qb < 3; qb++) rdx(0, qb);
#pragma unroll
    for (int i = 0; i < 8; i++) rda(0, i);
    static_for16([&](auto kc) {
      constexpr int kk = decltype(kc)::value;
      if (w < 3) wait_vm<qwait(kk, 6)>(); else wait_vm<qwait(kk, 0)>();
      asm volatile("s_barrier" ::: "memory");
#pragma unroll
      for (int i = 0; i < 8; i++) {
        if (kk < 15) {
          if (i < 3) rdx(kk + 1, i);
          rda(kk + 1, i);
        }
#pragma unroll
        for (int qb = 0; qb < 3; qb++) qa[i][qb] = mfma16(af[kk & 1][i], xf[kk & 1][qb], qa[i][qb]);
        if (kk + QNW < 16 && i < 3) ldw(kk + QNW, i);
        if ((kk & 1) && kk < 11 && i >= 3 && i < 6) ldx((kk + 5) / 2, i - 3);
        if (kk == KV0_STEP && i < 3) issue_group(i, 0, 0);
        if (kk == 15 && i >= 5) issue_group(i - 5, 1, 1);
        __builtin_amdgcn_sched_barrier(0);
      }
    });
    const bool odd = g >> 1;
#pragma unroll
    for (int qb = 0; qb < 3; qb++)
#pragma unroll
      for (int s = 0; s < 4; s++) {
        const unsigned a0 = pack2(qa[2 * s][qb][0], qa[2 * s][qb][1]), a1 = pack2(qa[2 * s][qb][2], qa[2 * s][qb][3]);
        const unsigned b0 = pack2(qa[2 * s + 1][qb][0], qa[2 * s + 1][qb][1]), b1 = pack2(qa[2 * s + 1][qb][2], qa[2 * s + 1][qb][3]);
        qf[qb][s] = u32x4{odd ? b0 : a0, odd ? b1 : a1, odd ? a0 : b0, odd ? a1 : b1};
      }
  }
#pragma unroll
  for (int e = 0; e < 3; e++) issue_group(e, 2, 2);

  const int strm = w < 2 ? w : 2;
  unsigned kaddr[4], vaddr0;
#pragma unroll
  for (int s = 0; s < 4; s++) kaddr[s] = strm * 16384 + r * 256 + (((4 * g + s) ^ (4 * (r & 3) + (r >> 2))) << 4);
  {
    const int row = 4 * g + (r >> 2), p = r & 3;
    vaddr0 = strm * 16384 + 8192 + row * 256 + ((((p >> 1)) ^ ((row & 7) << 1)) << 4) + (p & 1) * 8;
  }
  f32x4 oa[8][3];
#pragma unroll
  for (int i = 0; i < 8; i++)
#pragma unroll
    for (int qb = 0; qb < 3; qb++) oa[i][qb] = f32x4{0.f, 0.f, 0.f, 0.f};
  float mrow[3] = {-INFINITY, -INFINITY, -INFINITY}, lsum[3] = {0.f, 0.f, 0.f};

  asm volatile("s_waitcnt vmcnt(24) lgkmcnt(0)\n\ts_barrier" ::: "memory");
  int slot = 0;
  const int wu = ((ci * 4 + w) * 2 + ((xj >> 1) & 1));
  for (int t = 0; t < NT; t++) {
    const bool refill = t + 3 < NT;
    const char* sb = smem + slot * STAGE;
    u32x4 kf[2][4], vf[8];
#pragma unroll
    for (int s = 0; s < 4; s++)
#pragma unroll
      for (int kb = 0; kb < 2; kb++) kf[kb][s] = *(const u32x4*)(sb + kaddr[s] + kb * 4096);
    __builtin_amdgcn_sched_barrier(0);
    f32x4 sa[2][3];
#pragma unroll
    for (int s = 0; s < 4; s++) {
#pragma unroll
      for (int kb = 0; kb < 2; kb++)
#pragma unroll
        for (int qb = 0; qb < 3; qb++)
          sa[kb][qb] = mfma16(kf[kb][s], qf[qb][s], s == 0 ? f32x4{0.f, 0.f, 0.f, 0.f} : sa[kb][qb]);
      vf[2 * s] = tr_frag(sb + (vaddr0 ^ ((2 * s) << 5)), 4096);
      vf[2 * s + 1] = tr_frag(sb + (vaddr0 ^ ((2 * s + 1) << 5)), 4096);
      __builtin_amdgcn_sched_barrier(0);
    }
    asm volatile("s_waitcnt lgkmcnt(0)\n\ts_barrier" ::: "memory");
    if (refill) { issue_group(0, t + 3, slot); issue_group(1, t + 3, slot); }
    if (t == NT - 3) {
      __amdgpu_buffer_rsrc_t rp = mkrsrc(rfl_ptr((xj & 4 ? woB : woA) + (size_t)wu * 16384 + (xj & 1) * 512 - 3072), 18944);
      const unsigned pv = 3072 + (l >> 3) * 1024 + (l & 7) * 16;
      dma4(ldsbase + 2 * STAGE + w * 8192, rp, 0, pv, pv - 896, pv - 1792, pv - 2688);
      dma4(ldsbase + 2 * STAGE + w * 8192 + 4096, rp, 8192, pv, pv - 896, pv - 1792, pv - 2688);
    }
    float mx[3];
    bool need = false;
#pragma unroll
    for (int qb = 0; qb < 3; qb++) {
      float m0 = __builtin_fmaxf(__builtin_fmaxf(sa[0][qb][0], sa[0][qb][1]), sa[0][qb][2]);
      m0 = __builtin_fmaxf(__builtin_fmaxf(m0, sa[0][qb][3]), sa[1][qb][0]);
      m0 = __builtin_fmaxf(__builtin_fmaxf(m0, sa[1][qb][1]), sa[1][qb][2]);
      mx[qb] = __builtin_fmaxf(m0, sa[1][qb][3]);
      need |= mx[qb] > mrow[qb] + RESCALE_THR;
    }
    if (__builtin_expect(__builtin_amdgcn_ballot_w64(need) != 0, 0)) {
#pragma unroll
      for (int qb = 0; qb < 3; qb++) {
        float m = mx[qb];
        m = fmaxf(m, __shfl_xor(m, 16));
        m = fmaxf(m, __shfl_xor(m, 32));
        m = fmaxf(m, mrow[qb]);
        const float alpha = __builtin_amdgcn_exp2f((mrow[qb] - m) * SCL2);
        mrow[qb] = m;
        lsum[qb] *= alpha;
#pragma unroll
        for (int i = 0; i < 8; i++) scale_acc(oa[i][qb], alpha);
      }
    }
    u32x4 pf[3];
#pragma unroll
    for (int qb = 0; qb < 3; qb++) {
      const float mc = mrow[qb] * SCL2;
      float p[8];
#pragma unroll
      for (int kb = 0; kb < 2; kb++)
#pragma unroll
        for (int e = 0; e < 4; e++) p[kb * 4 + e] = __builtin_amdgcn_exp2f(__builtin_fmaf(sa[kb][qb][e], SCL2, -mc));
      lsum[qb] += ((p[0] + p[1]) + (p[2] + p[3])) + ((p[4] + p[5]) + (p[6] + p[7]));
#pragma unroll
      for (int e = 0; e < 4; e++) pf[qb][e] = pack2(p[2 * e], p[2 * e + 1]);
    }
    if (refill) issue_group(2, t + 3, slot);
#pragma unroll
    for (int qb = 0; qb < 3; qb++) {
#pragma unroll
      for (int db = 0; db < 8; db++) oa[db][qb] = mfma16(vf[db], pf[qb], oa[db][qb]);
    }
    slot = slot == 2 ? 0 : slot + 1;
    if (t >= NT - 2) asm volatile("s_waitcnt vmcnt(0) lgkmcnt(0)\n\ts_barrier" ::: "memory");
    else if (t == NT - 3) asm volatile("s_waitcnt vmcnt(20) lgkmcnt(0)\n\ts_barrier" ::: "memory");
    else asm volatile("s_waitcnt vmcnt(24) lgkmcnt(0)\n\ts_barrier" ::: "memory");
  }

  __amdgpu_buffer_rsrc_t rsw = mkrsrc(rfl_ptr((const char*)(Obuf + WPK_OFF + ((xj >> 2) * 8 + (xj & 1) * 4) * 32768 + wu * 128)),
                                      3 * 524288 + 2048);
#pragma unroll
  for (int i = 0; i < 8; i++)
    __builtin_amdgcn_raw_buffer_store_b128(*(const u32x4*)(smem + 2 * STAGE + w * 8192 + i * 1024 + l * 16), rsw, l * 16,
                                           (i & 3) * 524288 + (i >> 2) * 1024, WT);
  const int task = ((w < 2 ? 1 : 0) * NB + tb) * NH + th;
  __amdgpu_buffer_rsrc_t rso = mkrsrc(rfl_ptr((const char*)(Obuf + (size_t)task * 12 * 64)), 12 * 1024);
#pragma unroll
  for (int qb = 0; qb < 3; qb++) {
    float s = lsum[qb];
    s += __shfl_xor(s, 16);
    s += __shfl_xor(s, 32);
    const float inv = 1.0f / s;
#pragma unroll
    for (int u = 0; u < 4; u++) {
      u32x4 o;
      o[0] = pack2(oa[2 * u][qb][0] * inv, oa[2 * u][qb][1] * inv);
      o[1] = pack2(oa[2 * u][qb][2] * inv, oa[2 * u][qb][3] * inv);
      o[2] = pack2(oa[2 * u + 1][qb][0] * inv, oa[2 * u + 1][qb][1] * inv);
      o[3] = pack2(oa[2 * u + 1][qb][2] * inv, oa[2 * u + 1][qb][3] * inv);
      __builtin_amdgcn_raw_buffer_store_b128(o, rso, l * 16, (u * 3 + qb) * 1024, WT);
    }
  }
}

constexpr int OQD = 6;
constexpr int OP_LDS = 4 * OQD * 4096;
__global__ __launch_bounds__(256, 1) void oproj_kernel(const u32x4* __restrict__ Obuf, char* __restrict__ out) {
  extern __shared__ __attribute__((aligned(16))) char smem[];
  const int bid = blockIdx.x, xj = bid & 7, ci = bid >> 3;
  const int br = xj >> 2, b = 8 * ((xj >> 1) & 1) + (ci >> 2), ns = (4 * (xj & 1) + (ci & 3));
  const int w = __builtin_amdgcn_readfirstlane(threadIdx.x >> 6), l = threadIdx.x & 63;
  const int r = l & 15, g = l >> 4;
  const unsigned ring = (unsigned)(uintptr_t)(lds_char_p)smem + w * (OQD * 4096);
  const char* wp = rfl_ptr((const char*)(Obuf + WPK_OFF + (br * 8 + ns) * 32768 + w * 8192));
  __amdgpu_buffer_rsrc_t rw = mkrsrc(wp, 1024 * 128);
  const char* op = rfl_ptr((const char*)(Obuf + (size_t)((br * NB + b) * NH + w * 8) * 12 * 64));
  __amdgpu_buffer_rsrc_t ro = mkrsrc(op, 8 * 12 * 1024);
  const unsigned wvo = (l >> 3) * 128 + (((l & 7) ^ (((l >> 4) & 3) << 1)) << 4);
  const unsigned ovo = l * 16;
  u32x4 ob[OQD][3];
  f32x4 acc[4][3];
#pragma unroll
  for (int i = 0; i < 4; i++)
#pragma unroll
    for (int qb = 0; qb < 3; qb++) acc[i][qb] = f32x4{0.f, 0.f, 0.f, 0.f};
  auto ldw = [&](int kk, int sl) {
    asm volatile(
        "s_mov_b32 m0, %0\n\ts_nop 0\n\t"
        "buffer_load_dwordx4 %1, %5, %6 offen lds\n\t"
        "buffer_load_dwordx4 %2, %5, %6 offen offset:1024 lds\n\t"
        "buffer_load_dwordx4 %3, %5, %6 offen offset:2048 lds\n\t"
        "buffer_load_dwordx4 %4, %5, %6 offen offset:3072 lds"
        :: "s"(ring + sl * 4096), "v"(wvo), "v"(wvo), "v"(wvo), "v"(wvo), "s"(rw),
           "s"(kk * 4096)
        : "memory");
  };
  auto ldo = [&](int kk, int sl) {
    asm volatile(
        "buffer_load_dwordx4 %0, %3, %4, %5 offen\n\t"
        "buffer_load_dwordx4 %1, %3, %4, %5 offen offset:1024\n\t"
        "buffer_load_dwordx4 %2, %3, %4, %5 offen offset:2048"
        : "=&v"(ob[sl][0]), "=&v"(ob[sl][1]), "=&v"(ob[sl][2])
        : "v"(ovo), "s"(ro), "s"(kk * 3072)
        : "memory");
  };
  const int row = 4 * g + (r >> 2), p = r & 3;
  const unsigned aoff = w * (OQD * 4096) + row * 128 + (((p >> 1) ^ (((row >> 1) & 3) << 1)) << 4) + (p & 1) * 8;
#pragma unroll
  for (int kk = 0; kk < OQD; kk++) { ldw(kk, kk); ldo(kk, kk); }
  u32x4 af[2][4];
  asm volatile("s_waitcnt vmcnt(%0)" :: "n"(7 * OQD - 4) : "memory");
#pragma unroll
  for (int db = 0; db < 4; db++) af[0][db] = tr_frag(smem + (aoff ^ (db << 5)), 2048);
  auto step = [&](auto kc) {
    constexpr int kk = decltype(kc)::value, sl = kk % OQD;
    constexpr int last = kk + OQD - 1 < 31 ? kk + OQD - 1 : 31;
    constexpr int allow = kk < 31 ? 7 * (last - kk - 1) + 3 : 0;
    asm volatile("s_waitcnt vmcnt(%3) lgkmcnt(0)" : "+v"(ob[sl][0]), "+v"(ob[sl][1]), "+v"(ob[sl][2]) : "n"(allow) : "memory");
    if (kk < 31) {
#pragma unroll
      for (int db = 0; db < 4; db++) af[(kk + 1) & 1][db] = tr_frag(smem + ((kk + 1) % OQD) * 4096 + (aoff ^ (db << 5)), 2048);
    }
#pragma unroll
    for (int db = 0; db < 2; db++)
#pragma unroll
      for (int qb = 0; qb < 3; qb++) acc[db][qb] = mfma16(af[kk & 1][db], ob[sl][qb], acc[db][qb]);
    __builtin_amdgcn_sched_barrier(0);
    if (kk + OQD < 32) ldw(kk + OQD, sl);
#pragma unroll
    for (int db = 2; db < 4; db++)
#pragma unroll
      for (int qb = 0; qb < 3; qb++) acc[db][qb] = mfma16(af[kk & 1][db], ob[sl][qb], acc[db][qb]);
    __builtin_amdgcn_sched_barrier(0);
    if (kk + OQD < 32) ldo(kk + OQD, sl);
  };
  static_for16(step);
  static_for16([&](auto kc) { step(IC<16 + decltype(kc)::value>{}); });
  f32x4* red = (f32x4*)smem;
#pragma unroll
  for (int qb = 0; qb < 3; qb++)
#pragma unroll
    for (int db = 0; db < 4; db++) red[w * (OQD * 256) + (qb * 4 + db) * 64 + l] = acc[db][qb];
  __syncthreads();
  __amdgpu_buffer_rsrc_t rout = mkrsrc(rfl_ptr(out + (size_t)b * SQ * 2048 + br * 1024 + ns * 128), (SQ - 1) * 2048 + 128);
#pragma unroll
  for (int it = 0; it < 2; it++) {
    const int wv = w + 4 * it;
    if (wv < 6) {
      const int q = 8 * wv + (l >> 3), c8 = l & 7;
      const int qb = q >> 4, idx = (qb * 4 + (c8 >> 1)) * 64 + (2 * (c8 & 1)) * 16 + (q & 15);
      f32x4 s0 = {0.f, 0.f, 0.f, 0.f}, s1 = s0;
#pragma unroll
      for (int ww = 0; ww < 4; ww++) {
        s0 += red[ww * (OQD * 256) + idx];
        s1 += red[ww * (OQD * 256) + idx + 16];
      }
      u32x4 o = {pack2(s0[0], s0[1]), pack2(s0[2], s0[3]), pack2(s1[0], s1[1]), pack2(s1[2], s1[3])};
      __builtin_amdgcn_raw_buffer_store_b128(o, rout, q * 2048 + c8 * 16, 0, WT);
    }
  }
}

extern "C" void kernel_launch(void* const* d_in, const int* in_sizes, int n_in, void* d_out, int out_size,
                              void* d_ws, size_t ws_size, hipStream_t stream) {
  const char* xA = (const char*)d_in[0]; const char* KA = (const char*)d_in[1]; const char* VA = (const char*)d_in[2];
  const char* wqA = (const char*)d_in[3]; const char* woA = (const char*)d_in[4];
  const char* xB = (const char*)d_in[5]; const char* KB = (const char*)d_in[6]; const char* VB = (const char*)d_in[7];
  const char* wqB = (const char*)d_in[8]; const char* woB = (const char*)d_in[9];
  u32x4* Obuf = (u32x4*)d_ws;
  (void)hipFuncSetAttribute((const void*)attn_kernel, hipFuncAttributeMaxDynamicSharedMemorySize, ATTN_LDS);
  (void)hipFuncSetAttribute((const void*)oproj_kernel, hipFuncAttributeMaxDynamicSharedMemorySize, OP_LDS);
  attn_kernel<<<256, 256, ATTN_LDS, stream>>>(xA, KA, VA, wqA, xB, KB, VB, wqB, woA, woB, Obuf);
  oproj_kernel<<<256, 256, OP_LDS, stream>>>(Obuf, (char*)d_out);
}
```
